```python
import torch

def _softmax(x, axis=-1):
    return torch.softmax(x, dim=axis)
def _gelu(x):
    return torch.nn.functional.gelu(x, approximate="tanh")
def _silu(x):
    return torch.nn.functional.silu(x)
def _sigmoid(x):
    return torch.sigmoid(x)
def _layernorm(x, axis=-1, eps=1e-5):
    m = x.mean(dim=axis, keepdim=True)
    v = x.var(dim=axis, keepdim=True, unbiased=False)
    return (x - m) / torch.sqrt(v + eps)
def _scatter_add(tgt, idx, src, axis=0):
    return tgt.clone().index_add_(axis, idx, src)
def _segment_sum(src, seg, n):
    out = torch.zeros((n,) + src.shape[1:], dtype=src.dtype)
    return out.index_add_(0, seg, src)
def _pad(x, pw):
    flat = tuple(v for pair in reversed(pw) for v in pair)
    return torch.nn.functional.pad(x, flat)

kernel_name = 'standalone__structured_sparse_n_m__ragged'

def setup_inputs():

    _w = (lambda a: (lambda t: t.to(torch.bfloat16) if t.is_floating_point() else t)(torch.as_tensor(a)))
    B, S, D = 4, 2048, 576
    assert D % 4 == 0, "d_model must be divisible by M=4 for 2:4 sparsity"
    return {
        "x": _w((lambda *s: torch.randn(*s, dtype=torch.bfloat16) * 0.05)(B, S, D)),
        "w": _w((lambda *s: torch.randn(*s, dtype=torch.bfloat16) * 0.05)(D, D)),
        "b": _w((lambda *s: torch.randn(*s, dtype=torch.bfloat16) * 0.05)(D)),
    }

def reference(x, w, b):
    B, S, D = x.shape
    N, M = 2, 4
    G = D // M
    wg = w.reshape(G, M, D)
    mag = torch.abs(wg)
    order = torch.argsort(mag, 1)
    thr = (lambda x, i, axis: torch.take_along_dim(x, i, dim=axis))(mag, order[:, M - N:M - N + 1, :], 1)
    mask = (lambda x, dt: x.to(dt))(mag >= thr, w.dtype)
    wp = (wg * mask).reshape(D, D)
    return torch.einsum("bsd,df->bsf", x, wp) + b
```

```cpp
#include <hip/hip_runtime.h>
#include <hip/hip_bf16.h>
#include <stdint.h>
using bf16x8 = __attribute__((ext_vector_type(8))) short;
using f32x4  = __attribute__((ext_vector_type(4))) float;

__global__ void prune_k(const uint16_t* __restrict__ w, uint16_t* __restrict__ wt, int D) {
  int idx = blockIdx.x * blockDim.x + threadIdx.x;
  int G = D / 4;
  if (idx >= G * D) return;
  int g = idx / D, f = idx % D;
  uint16_t v[4]; uint16_t m[4];
  for (int i = 0; i < 4; ++i) { v[i] = w[(4 * g + i) * D + f]; m[i] = v[i] & 0x7fff; }
  uint16_t hi1 = max(m[0], m[1]), lo1 = min(m[0], m[1]), hi2 = max(m[2], m[3]), lo2 = min(m[2], m[3]);
  uint16_t thr = max(min(hi1, hi2), max(lo1, lo2));
  for (int i = 0; i < 4; ++i) wt[f * D + 4 * g + i] = m[i] >= thr ? v[i] : 0;
}

__device__ inline float bf2f(uint16_t v) { return __uint_as_float((uint32_t)v << 16); }
__device__ inline uint16_t f2bf(float f) { __hip_bfloat16 b = __float2bfloat16(f); return *(uint16_t*)&b; }

__global__ __launch_bounds__(64) void gemm_k(const uint16_t* __restrict__ x, const uint16_t* __restrict__ wt, const uint16_t* __restrict__ b, uint16_t* __restrict__ out, int D) {
  int bn = blockIdx.x * 16, bm = blockIdx.y * 16;
  int lane = threadIdx.x, r = lane & 15, q = lane >> 4;
  f32x4 acc = {0, 0, 0, 0};
  for (int k = 0; k < D; k += 32) {
    bf16x8 a = *(const bf16x8*)(x + (size_t)(bm + r) * D + k + q * 8);
    bf16x8 bb = *(const bf16x8*)(wt + (size_t)(bn + r) * D + k + q * 8);
    acc = __builtin_amdgcn_mfma_f32_16x16x32_bf16(a, bb, acc, 0, 0, 0);
  }
  for (int j = 0; j < 4; ++j) {
    float v = bf2f(f2bf(acc[j])) + bf2f(b[bn + r]);
    out[(size_t)(bm + q * 4 + j) * D + bn + r] = f2bf(v);
  }
}

extern "C" void kernel_launch(void* const* d_in, const int* in_sizes, int n_in,
                              void* d_out, int out_size, void* d_ws, size_t ws_size,
                              hipStream_t stream) {
  const uint16_t* x = (const uint16_t*)d_in[0];
  const uint16_t* w = (const uint16_t*)d_in[1];
  const uint16_t* b = (const uint16_t*)d_in[2];
  int D = in_sizes[2];
  int M = in_sizes[0] / D;
  uint16_t* wt = (uint16_t*)d_ws;
  prune_k<<<(D / 4 * D + 255) / 256, 256, 0, stream>>>(w, wt, D);
  gemm_k<<<dim3(D / 16, M / 16), 64, 0, stream>>>(x, wt, b, (uint16_t*)d_out, D);
}
```

```cpp
#include <hip/hip_runtime.h>
#include <hip/hip_bf16.h>
#include <stdint.h>
using bf16x8 = __attribute__((ext_vector_type(8))) short;
using f32x4  = __attribute__((ext_vector_type(4))) float;
#define BIGLDS 1
#define THREADS 512
__device__ inline float bf2f(uint16_t v) { return __uint_as_float((uint32_t)v << 16); }
__device__ inline uint16_t f2bf(float f) { __hip_bfloat16 b = __float2bfloat16(f); return *(uint16_t*)&b; }
__global__ __launch_bounds__(THREADS) void k(const uint16_t* __restrict__ x, const uint16_t* __restrict__ w, const uint16_t* __restrict__ b, uint16_t* __restrict__ out) {
  unsigned long long t0 = __builtin_amdgcn_s_memrealtime();
  __shared__ char smem[163840];
  if (threadIdx.x == 1000) out[0] = smem[x[0]];
  smem[threadIdx.x] = 1;
  const int D = 576, lane = threadIdx.x & 63, wid = threadIdx.x >> 6, r = lane & 15, q = lane >> 4;
  const int bm = blockIdx.x * 32;
  for (int cb = wid; cb < 36; cb += THREADS / 64) {
    f32x4 a0 = {0, 0, 0, 0}, a1 = {0, 0, 0, 0};
    for (int k0 = 0; k0 < D; k0 += 32) {
      bf16x8 xa = *(const bf16x8*)(x + (size_t)(bm + r) * D + k0 + q * 8), xb = *(const bf16x8*)(x + (size_t)(bm + 16 + r) * D + k0 + q * 8), wf;
      for (int g = 0; g < 2; ++g) {
        uint16_t v[4], m[4];
        for (int i = 0; i < 4; ++i) { v[i] = w[(k0 + q * 8 + g * 4 + i) * D + cb * 16 + r]; m[i] = v[i] & 0x7fff; }
        uint16_t hi1 = max(m[0], m[1]), lo1 = min(m[0], m[1]), hi2 = max(m[2], m[3]), lo2 = min(m[2], m[3]);
        uint16_t thr = max(min(hi1, hi2), max(lo1, lo2));
        for (int i = 0; i < 4; ++i) wf[g * 4 + i] = m[i] >= thr ? v[i] : 0;
      }
      a0 = __builtin_amdgcn_mfma_f32_16x16x32_bf16(xa, wf, a0, 0, 0, 0);
      a1 = __builtin_amdgcn_mfma_f32_16x16x32_bf16(xb, wf, a1, 0, 0, 0);
    }
    float bb = bf2f(b[cb * 16 + r]);
    for (int j = 0; j < 4; ++j) { out[(size_t)(bm + q * 4 + j) * D + cb * 16 + r] = f2bf(a0[j] + bb); out[(size_t)(bm + 16 + q * 4 + j) * D + cb * 16 + r] = f2bf(a1[j] + bb); }
  }
  while (__builtin_amdgcn_s_memrealtime() - t0 < 30000) __builtin_amdgcn_s_sleep(2);
}
extern "C" void kernel_launch(void* const* d_in, const int* in_sizes, int n_in, void* d_out, int out_size, void* d_ws, size_t ws_size, hipStream_t stream) {
  k<<<256, THREADS, 0, stream>>>((const uint16_t*)d_in[0], (const uint16_t*)d_in[1], (const uint16_t*)d_in[2], (uint16_t*)d_out);
}
```

```cpp
#include <hip/hip_runtime.h>
#include <stdint.h>

__global__ __launch_bounds__(512, 2) void spmm24_kernel(const uint16_t* __restrict__ x, const uint16_t* __restrict__ w, const uint16_t* __restrict__ bias,
                                                        uint16_t* __restrict__ out) {
  __shared__ __attribute__((aligned(16))) char smem[163840];
  unsigned tid = threadIdx.x;
  asm volatile(
    ".p2align 3\n"
    ".Lstart%=:\n"
    "s_load_dwordx8 s[4:11], %1, 0x0\n"
    "v_readfirstlane_b32 s27, %0\n"
    "s_and_b32 s28, %2, 7\n"
    "s_lshr_b32 s29, %2, 3\n"
    "v_and_b32_e64 v1, 63, %0\n"
    "s_lshr_b32 s27, s27, 6\n"
    "s_cmp_lt_u32 s29, 26\n"
    "s_cbranch_scc0 .Ltile_b%=\n"
    "s_mul_i32 s28, s28, 26\n"
    "s_add_u32 s28, s28, s29\n"
    "s_lshr_b32 s29, s28, 2\n"
    "s_and_b32 s28, s28, 3\n"
    "s_mul_i32 s24, s29, 184320\n"
    "s_lshl_b32 s25, s28, 8\n"
    "s_lshl_b32 s28, s28, 1\n"
    "s_add_u32 s28, s28, s29\n"
    "s_mov_b32 s37, 15\n"
    "s_branch .Lrot%=\n"
    ".Ltile_b%=:\n"
    "s_mov_b32 s30, 0x100b0500\n"
    "s_mov_b32 s31, 0x26201b16\n"
    "s_lshl_b32 s28, s28, 3\n"
    "s_lshr_b64 s[30:31], s[30:31], s28\n"
    "s_bfe_u32 s31, s30, 0x80008\n"
    "s_and_b32 s30, s30, 0xff\n"
    "s_cmp_eq_u32 s31, 0\n"
    "s_cselect_b32 s31, 43, s31\n"
    "s_add_u32 s30, s30, s29\n"
    "s_sub_u32 s30, s30, 26\n"
    "s_cmp_lt_u32 s30, s31\n"
    "s_cbranch_scc0 .Lend%=\n"
    "s_mul_i32 s24, s30, 221184\n"
    "s_movk_i32 s25, 0x400\n"
    "s_add_u32 s28, s30, 8\n"
    "s_mov_b32 s37, 7\n"
    ".Lrot%=:\n"
    "s_mul_i32 s29, s28, 57\n"
    "s_lshr_b32 s29, s29, 9\n"
    "s_mul_i32 s29, s29, 9\n"
    "s_sub_u32 s26, s28, s29\n"
    "s_cmp_lt_u32 s27, 4\n"
    "s_cbranch_scc0 .Lcons%=\n"
    "v_lshrrev_b32 v10, 4, v1\n"
    "v_lshlrev_b32 v11, 2, v10\n"
    "v_xor_b32 v11, v11, v1\n"
    "v_and_b32 v11, s37, v11\n"
    "v_lshlrev_b32 v11, 4, v11\n"
    "v_add_u32 v11, s25, v11\n"
    "s_lshl_b32 s28, s27, 4\n"
    "v_add_u32_e64 v10, s28, v10\n"
    "s_movk_i32 s29, 0x480\n"
    "v_mad_u32_u24 v8, v10, s29, v11\n"
    "s_mul_i32 s41, s26, 0x12000\n"
    "s_lshl_b32 s38, s27, 12\n"
    "s_add_u32 s38, s38, %3\n"
    "s_mov_b32 s22, 0xa2000\n"
    "s_mov_b32 s23, 0x20000\n"
    "v_lshrrev_b32 v10, 3, v1\n"
    "s_lshl_b32 s28, s27, 3\n"
    "v_add_u32 v10, s28, v10\n"
    "v_lshrrev_b32 v11, 1, v10\n"
    "v_xor_b32 v11, v11, v1\n"
    "v_and_b32 v11, 7, v11\n"
    "v_lshlrev_b32_e64 v11, 4, v11\n"
    "v_mad_u32_u24 v9, v10, s29, v11\n"
    "s_lshl_b32 s50, s26, 7\n"
    "s_add_u32 s50, s50, s24\n"
    "s_add_u32 s34, s24, 0x480\n"
    "s_mov_b32 s14, 0x900000\n"
    "s_mov_b32 s15, 0x20000\n"
    "s_mov_b32 m0, s38\n"
    "s_waitcnt lgkmcnt(0)\n"
    "s_mov_b32 s20, s6\n"
    "s_and_b32 s21, s7, 0xffff\n"
    "s_mov_b32 s12, s4\n"
    "s_and_b32 s13, s5, 0xffff\n"
    "s_cmp_eq_u32 s37, 15\n"
    "s_cbranch_scc0 .Lhelp_b%=\n"
    "s_mul_i32 s35, s27, 0x1000\n"
    "buffer_load_dwordx4 v8, s[20:23], s41 offen lds\n"
    "s_add_u32 s31, s41, 0xe00\n"
    "buffer_load_dwordx4 v8, s[20:23], s31 offen offset:1024 lds\n"
    "s_add_u32 s31, s41, 0x1c00\n"
    "buffer_load_dwordx4 v8, s[20:23], s31 offen offset:2048 lds\n"
    "s_add_u32 s31, s41, 0x2a00\n"
    "buffer_load_dwordx4 v8, s[20:23], s31 offen offset:3072 lds\n"
    "s_add_u32 s35, s35, %3\n"
    "s_add_u32 s42, s41, 0x12000\n"
    "s_cmp_eq_u32 s42, 0xa2000\n"
    "s_cselect_b32 s42, 0, s42\n"
    "s_add_u32 s51, s50, 0x80\n"
    "s_cmp_eq_u32 s51, s34\n"
    "s_cselect_b32 s51, s24, s51\n"
    "s_add_u32 m0, s38, 0x4000\n"
    "s_nop 0\n"
    "buffer_load_dwordx4 v8, s[20:23], s42 offen lds\n"
    "s_add_u32 s31, s42, 0xe00\n"
    "buffer_load_dwordx4 v8, s[20:23], s31 offen offset:1024 lds\n"
    "s_add_u32 s31, s42, 0x1c00\n"
    "buffer_load_dwordx4 v8, s[20:23], s31 offen offset:2048 lds\n"
    "s_add_u32 s31, s42, 0x2a00\n"
    "buffer_load_dwordx4 v8, s[20:23], s31 offen offset:3072 lds\n"
    "s_add_u32 m0, s35, 0x18000\n"
    "s_nop 0\n"
    "buffer_load_dwordx4 v9, s[12:15], s50 offen lds\n"
    "s_add_u32 s31, s50, 0x8c00\n"
    "buffer_load_dwordx4 v9, s[12:15], s31 offen offset:1024 lds\n"
    "s_add_u32 s31, s50, 0x11800\n"
    "buffer_load_dwordx4 v9, s[12:15], s31 offen offset:2048 lds\n"
    "s_add_u32 s31, s50, 0x1a400\n"
    "buffer_load_dwordx4 v9, s[12:15], s31 offen offset:3072 lds\n"
    "v_bfe_u32 v10, v1, 2, 2\n"
    "v_xor_b32 v11, s27, v10\n"
    "v_lshlrev_b32 v11, 6, v11\n"
    "v_lshl_add_u32 v11, v10, 8, v11\n"
    "v_lshrrev_b32_e64 v10, 5, v1\n"
    "v_lshl_add_u32 v11, v10, 11, v11\n"
    "v_and_b32_e64 v10, 1, v1\n"
    "v_lshl_add_u32 v11, v10, 5, v11\n"
    "v_and_b32 v10, 16, v1\n"
    "v_add_u32 v11, v11, v10\n"
    "v_bfe_u32 v10, v1, 1, 1\n"
    "v_lshl_add_u32 v11, v10, 3, v11\n"
    "v_add_u32_e64 v2, %3, v11\n"
    "s_mov_b32 s39, 0x7fff7fff\n"
    "s_mul_i32 s28, s27, 1024\n"
    "s_add_u32 s28, s28, %3\n"
    "s_add_u32 s28, s28, 0x8000\n"
    "v_lshlrev_b32 v3, 4, v1\n"
    "v_add_u32 v3, s28, v3\n"
    "s_lshl_b32 s28, s27, 6\n"
    "s_add_u32 s25, s25, s28\n"
    "s_mov_b32 s36, s24\n"
    "s_lshl_b32 s28, s27, 11\n"
    "s_add_u32 s28, s28, %3\n"
    "v_lshrrev_b32 v10, 3, v1\n"
    "v_xor_b32 v10, v10, v1\n"
    "v_and_b32 v10, 3, v10\n"
    "v_lshlrev_b32 v10, 4, v10\n"
    "v_lshrrev_b32_e64 v11, 2, v1\n"
    "v_lshl_add_u32 v10, v11, 6, v10\n"
    "v_add_u32_e64 v4, s28, v10\n"
    "v_mul_u32_u24 v11, 0x480, v11\n"
    "v_and_b32_e64 v10, 3, v1\n"
    "v_lshl_add_u32 v11, v10, 4, v11\n"
    "v_add_u32 v5, s25, v11\n"
    "s_mov_b32 s16, s10\n"
    "s_and_b32 s17, s11, 0xffff\n"
    "s_mov_b32 s18, 0x900000\n"
    "s_mov_b32 s19, 0x20000\n"
    "s_lshl_b32 s28, s27, 4\n"
    "s_add_u32 s28, s28, %3\n"
    "s_add_u32 s28, s28, 0xff00\n"
    "v_mov_b32_e64 v6, s28\n"
    "s_add_u32 s43, s42, 0x12000\n"
    "s_cmp_eq_u32 s43, 0xa2000\n"
    "s_cselect_b32 s43, 0, s43\n"
    "s_add_u32 s52, s51, 0x80\n"
    "s_cmp_eq_u32 s52, s34\n"
    "s_cselect_b32 s52, s24, s52\n"
    "s_add_u32 m0, s38, 0x8000\n"
    "s_nop 0\n"
    "buffer_load_dwordx4 v8, s[20:23], s43 offen lds\n"
    "s_add_u32 s31, s43, 0xe00\n"
    "buffer_load_dwordx4 v8, s[20:23], s31 offen offset:1024 lds\n"
    "s_add_u32 s31, s43, 0x1c00\n"
    "buffer_load_dwordx4 v8, s[20:23], s31 offen offset:2048 lds\n"
    "s_add_u32 s31, s43, 0x2a00\n"
    "buffer_load_dwordx4 v8, s[20:23], s31 offen offset:3072 lds\n"
    "s_add_u32 m0, s35, 0x1c000\n"
    "s_nop 0\n"
    "buffer_load_dwordx4 v9, s[12:15], s51 offen lds\n"
    "s_add_u32 s31, s51, 0x8c00\n"
    "buffer_load_dwordx4 v9, s[12:15], s31 offen offset:1024 lds\n"
    "s_add_u32 s31, s51, 0x11800\n"
    "buffer_load_dwordx4 v9, s[12:15], s31 offen offset:2048 lds\n"
    "s_add_u32 s31, s51, 0x1a400\n"
    "buffer_load_dwordx4 v9, s[12:15], s31 offen offset:3072 lds\n"
    "s_waitcnt vmcnt(16)\n"
    "s_barrier\n"
    "ds_read_b64_tr_b16 v[16:17], v2 offset:0\n"
    "ds_read_b64_tr_b16 v[18:19], v2 offset:1024\n"
    "s_waitcnt lgkmcnt(0)\n"
    "v_and_b32 v48, s39, v16\n"
    "v_and_b32 v49, s39, v17\n"
    "v_and_b32 v50, s39, v18\n"
    "v_and_b32 v51, s39, v19\n"
    "v_pk_max_u16 v52, v48, v49\n"
    "v_pk_max_u16 v53, v50, v51\n"
    "v_pk_min_u16 v54, v48, v49\n"
    "v_pk_min_u16 v55, v50, v51\n"
    "v_pk_max_u16 v52, v52, v54 op_sel:[0,1] op_sel_hi:[1,0]\n"
    "v_pk_max_u16 v53, v53, v55 op_sel:[0,1] op_sel_hi:[1,0]\n"
    "v_pk_min_u16 v52, v52, v52 op_sel:[0,1] op_sel_hi:[1,0]\n"
    "v_pk_min_u16 v53, v53, v53 op_sel:[0,1] op_sel_hi:[1,0]\n"
    "v_pk_sub_i16 v48, v48, v52\n"
    "v_pk_sub_i16 v49, v49, v52\n"
    "v_pk_sub_i16 v50, v50, v53\n"
    "v_pk_sub_i16 v51, v51, v53\n"
    "v_pk_ashrrev_i16 v48, 15, v48 op_sel_hi:[0,1]\n"
    "v_pk_ashrrev_i16 v49, 15, v49 op_sel_hi:[0,1]\n"
    "v_pk_ashrrev_i16 v50, 15, v50 op_sel_hi:[0,1]\n"
    "v_pk_ashrrev_i16 v51, 15, v51 op_sel_hi:[0,1]\n"
    "v_bitop3_b32 v16, v16, v48, v16 bitop3:0x30\n"
    "v_bitop3_b32 v17, v17, v49, v17 bitop3:0x30\n"
    "v_bitop3_b32 v18, v18, v50, v18 bitop3:0x30\n"
    "v_bitop3_b32 v19, v19, v51, v19 bitop3:0x30\n"
    "ds_write_b128 v3, v[16:19] offset:32768\n"
    "s_waitcnt vmcnt(8) lgkmcnt(0)\n"
    "s_barrier\n"
    "ds_read_b64_tr_b16 v[16:17], v2 offset:16384\n"
    "ds_read_b64_tr_b16 v[18:19], v2 offset:17408\n"
    "s_add_u32 s44, s43, 0x12000\n"
    "s_cmp_eq_u32 s44, 0xa2000\n"
    "s_cselect_b32 s44, 0, s44\n"
    "s_add_u32 s53, s52, 0x80\n"
    "s_cmp_eq_u32 s53, s34\n"
    "s_cselect_b32 s53, s24, s53\n"
    "s_add_u32 m0, s38, 0xc000\n"
    "s_nop 0\n"
    "buffer_load_dwordx4 v8, s[20:23], s44 offen lds\n"
    "s_waitcnt lgkmcnt(0)\n"
    "v_and_b32 v48, s39, v16\n"
    "v_and_b32 v49, s39, v17\n"
    "v_and_b32 v50, s39, v18\n"
    "v_and_b32_e64 v51, s39, v19\n"
    "v_pk_max_u16 v52, v48, v49\n"
    "s_add_u32 s31, s44, 0xe00\n"
    "buffer_load_dwordx4 v8, s[20:23], s31 offen offset:1024 lds\n"
    "v_pk_max_u16 v53, v50, v51\n"
    "v_pk_min_u16 v54, v48, v49\n"
    "v_pk_min_u16 v55, v50, v51\n"
    "v_pk_max_u16 v52, v52, v54 op_sel:[0,1] op_sel_hi:[1,0]\n"
    "v_pk_max_u16 v53, v53, v55 op_sel:[0,1] op_sel_hi:[1,0]\n"
    "v_pk_min_u16 v52, v52, v52 op_sel:[0,1] op_sel_hi:[1,0]\n"
    "s_add_u32 s31, s44, 0x1c00\n"
    "buffer_load_dwordx4 v8, s[20:23], s31 offen offset:2048 lds\n"
    "v_pk_min_u16 v53, v53, v53 op_sel:[0,1] op_sel_hi:[1,0]\n"
    "v_pk_sub_i16 v48, v48, v52\n"
    "v_pk_sub_i16 v49, v49, v52\n"
    "v_pk_sub_i16 v50, v50, v53\n"
    "v_pk_sub_i16 v51, v51, v53\n"
    "v_pk_ashrrev_i16 v48, 15, v48 op_sel_hi:[0,1]\n"
    "s_add_u32 s31, s44, 0x2a00\n"
    "buffer_load_dwordx4 v8, s[20:23], s31 offen offset:3072 lds\n"
    "v_pk_ashrrev_i16 v49, 15, v49 op_sel_hi:[0,1]\n"
    "v_pk_ashrrev_i16 v50, 15, v50 op_sel_hi:[0,1]\n"
    "v_pk_ashrrev_i16 v51, 15, v51 op_sel_hi:[0,1]\n"
    "v_bitop3_b32 v16, v16, v48, v16 bitop3:0x30\n"
    "v_bitop3_b32 v17, v17, v49, v17 bitop3:0x30\n"
    "v_bitop3_b32 v18, v18, v50, v18 bitop3:0x30\n"
    "s_add_u32 m0, s35, 0x20000\n"
    "s_nop 0\n"
    "buffer_load_dwordx4 v9, s[12:15], s52 offen lds\n"
    "v_bitop3_b32 v19, v19, v51, v19 bitop3:0x30\n"
    "ds_write_b128 v3, v[16:19] offset:36864\n"
    "s_add_u32 s31, s52, 0x8c00\n"
    "buffer_load_dwordx4 v9, s[12:15], s31 offen offset:1024 lds\n"
    "s_add_u32 s31, s52, 0x11800\n"
    "buffer_load_dwordx4 v9, s[12:15], s31 offen offset:2048 lds\n"
    "s_add_u32 s31, s52, 0x1a400\n"
    "buffer_load_dwordx4 v9, s[12:15], s31 offen offset:3072 lds\n"
    "s_waitcnt vmcnt(8) lgkmcnt(0)\n"
    "s_barrier\n"
    "ds_read_b64_tr_b16 v[16:17], v2 offset:32768\n"
    "ds_read_b64_tr_b16 v[18:19], v2 offset:33792\n"
    "s_add_u32 s45, s44, 0x12000\n"
    "s_cmp_eq_u32 s45, 0xa2000\n"
    "s_cselect_b32 s45, 0, s45\n"
    "s_add_u32 s54, s53, 0x80\n"
    "s_cmp_eq_u32 s54, s34\n"
    "s_cselect_b32 s54, s24, s54\n"
    "s_add_u32 m0, s38, 0x0\n"
    "s_nop 0\n"
    "buffer_load_dwordx4 v8, s[20:23], s45 offen lds\n"
    "s_waitcnt lgkmcnt(0)\n"
    "v_and_b32 v48, s39, v16\n"
    "v_and_b32 v49, s39, v17\n"
    "v_and_b32 v50, s39, v18\n"
    "v_and_b32_e64 v51, s39, v19\n"
    "v_pk_max_u16 v52, v48, v49\n"
    "s_add_u32 s31, s45, 0xe00\n"
    "buffer_load_dwordx4 v8, s[20:23], s31 offen offset:1024 lds\n"
    "v_pk_max_u16 v53, v50, v51\n"
    "v_pk_min_u16 v54, v48, v49\n"
    "v_pk_min_u16 v55, v50, v51\n"
    "v_pk_max_u16 v52, v52, v54 op_sel:[0,1] op_sel_hi:[1,0]\n"
    "v_pk_max_u16 v53, v53, v55 op_sel:[0,1] op_sel_hi:[1,0]\n"
    "v_pk_min_u16 v52, v52, v52 op_sel:[0,1] op_sel_hi:[1,0]\n"
    "s_add_u32 s31, s45, 0x1c00\n"
    "buffer_load_dwordx4 v8, s[20:23], s31 offen offset:2048 lds\n"
    "v_pk_min_u16 v53, v53, v53 op_sel:[0,1] op_sel_hi:[1,0]\n"
    "v_pk_sub_i16 v48, v48, v52\n"
    "v_pk_sub_i16 v49, v49, v52\n"
    "v_pk_sub_i16 v50, v50, v53\n"
    "v_pk_sub_i16 v51, v51, v53\n"
    "v_pk_ashrrev_i16 v48, 15, v48 op_sel_hi:[0,1]\n"
    "s_add_u32 s31, s45, 0x2a00\n"
    "buffer_load_dwordx4 v8, s[20:23], s31 offen offset:3072 lds\n"
    "v_pk_ashrrev_i16 v49, 15, v49 op_sel_hi:[0,1]\n"
    "v_pk_ashrrev_i16 v50, 15, v50 op_sel_hi:[0,1]\n"
    "v_pk_ashrrev_i16 v51, 15, v51 op_sel_hi:[0,1]\n"
    "v_bitop3_b32 v16, v16, v48, v16 bitop3:0x30\n"
    "v_bitop3_b32 v17, v17, v49, v17 bitop3:0x30\n"
    "v_bitop3_b32 v18, v18, v50, v18 bitop3:0x30\n"
    "s_add_u32 m0, s35, 0x24000\n"
    "s_nop 0\n"
    "buffer_load_dwordx4 v9, s[12:15], s53 offen lds\n"
    "v_bitop3_b32 v19, v19, v51, v19 bitop3:0x30\n"
    "ds_write_b128 v3, v[16:19] offset:32768\n"
    "s_add_u32 s31, s53, 0x8c00\n"
    "buffer_load_dwordx4 v9, s[12:15], s31 offen offset:1024 lds\n"
    "s_add_u32 s31, s53, 0x11800\n"
    "buffer_load_dwordx4 v9, s[12:15], s31 offen offset:2048 lds\n"
    "s_add_u32 s31, s53, 0x1a400\n"
    "buffer_load_dwordx4 v9, s[12:15], s31 offen offset:3072 lds\n"
    "s_waitcnt vmcnt(8) lgkmcnt(0)\n"
    "s_barrier\n"
    "ds_read_b64_tr_b16 v[16:17], v2 offset:49152\n"
    "ds_read_b64_tr_b16 v[18:19], v2 offset:50176\n"
    "s_add_u32 s46, s45, 0x12000\n"
    "s_cmp_eq_u32 s46, 0xa2000\n"
    "s_cselect_b32 s46, 0, s46\n"
    "s_add_u32 s55, s54, 0x80\n"
    "s_cmp_eq_u32 s55, s34\n"
    "s_cselect_b32 s55, s24, s55\n"
    "s_add_u32 m0, s38, 0x4000\n"
    "s_nop 0\n"
    "buffer_load_dwordx4 v8, s[20:23], s46 offen lds\n"
    "s_waitcnt lgkmcnt(0)\n"
    "v_and_b32 v48, s39, v16\n"
    "v_and_b32 v49, s39, v17\n"
    "v_and_b32 v50, s39, v18\n"
    "v_and_b32 v51, s39, v19\n"
    "v_pk_max_u16 v52, v48, v49\n"
    "s_add_u32 s31, s46, 0xe00\n"
    "buffer_load_dwordx4 v8, s[20:23], s31 offen offset:1024 lds\n"
    "v_pk_max_u16 v53, v50, v51\n"
    "v_pk_min_u16 v54, v48, v49\n"
    "v_pk_min_u16 v55, v50, v51\n"
    "v_pk_max_u16 v52, v52, v54 op_sel:[0,1] op_sel_hi:[1,0]\n"
    "v_pk_max_u16 v53, v53, v55 op_sel:[0,1] op_sel_hi:[1,0]\n"
    "v_pk_min_u16 v52, v52, v52 op_sel:[0,1] op_sel_hi:[1,0]\n"
    "s_add_u32 s31, s46, 0x1c00\n"
    "buffer_load_dwordx4 v8, s[20:23], s31 offen offset:2048 lds\n"
    "v_pk_min_u16 v53, v53, v53 op_sel:[0,1] op_sel_hi:[1,0]\n"
    "v_pk_sub_i16 v48, v48, v52\n"
    "v_pk_sub_i16 v49, v49, v52\n"
    "v_pk_sub_i16 v50, v50, v53\n"
    "v_pk_sub_i16 v51, v51, v53\n"
    "v_pk_ashrrev_i16 v48, 15, v48 op_sel_hi:[0,1]\n"
    "s_add_u32 s31, s46, 0x2a00\n"
    "buffer_load_dwordx4 v8, s[20:23], s31 offen offset:3072 lds\n"
    "v_pk_ashrrev_i16 v49, 15, v49 op_sel_hi:[0,1]\n"
    "v_pk_ashrrev_i16 v50, 15, v50 op_sel_hi:[0,1]\n"
    "v_pk_ashrrev_i16 v51, 15, v51 op_sel_hi:[0,1]\n"
    "v_bitop3_b32 v16, v16, v48, v16 bitop3:0x30\n"
    "v_bitop3_b32 v17, v17, v49, v17 bitop3:0x30\n"
    "v_bitop3_b32 v18, v18, v50, v18 bitop3:0x30\n"
    "s_add_u32 m0, s35, 0x18000\n"
    "s_nop 0\n"
    "buffer_load_dwordx4 v9, s[12:15], s54 offen lds\n"
    "v_bitop3_b32 v19, v19, v51, v19 bitop3:0x30\n"
    "ds_write_b128 v3, v[16:19] offset:36864\n"
    "s_add_u32 s31, s54, 0x8c00\n"
    "buffer_load_dwordx4 v9, s[12:15], s31 offen offset:1024 lds\n"
    "s_add_u32 s31, s54, 0x11800\n"
    "buffer_load_dwordx4 v9, s[12:15], s31 offen offset:2048 lds\n"
    "s_add_u32 s31, s54, 0x1a400\n"
    "buffer_load_dwordx4 v9, s[12:15], s31 offen offset:3072 lds\n"
    "s_waitcnt vmcnt(8) lgkmcnt(0)\n"
    "s_barrier\n"
    "ds_read_b64_tr_b16 v[16:17], v2 offset:0\n"
    "ds_read_b64_tr_b16 v[18:19], v2 offset:1024\n"
    "s_add_u32 s47, s46, 0x12000\n"
    "s_cmp_eq_u32 s47, 0xa2000\n"
    "s_cselect_b32 s47, 0, s47\n"
    "s_add_u32 s56, s55, 0x80\n"
    "s_cmp_eq_u32 s56, s34\n"
    "s_cselect_b32 s56, s24, s56\n"
    "s_add_u32 m0, s38, 0x8000\n"
    "s_nop 0\n"
    "buffer_load_dwordx4 v8, s[20:23], s47 offen lds\n"
    "s_waitcnt lgkmcnt(0)\n"
    "v_and_b32 v48, s39, v16\n"
    "v_and_b32 v49, s39, v17\n"
    "v_and_b32 v50, s39, v18\n"
    "v_and_b32 v51, s39, v19\n"
    "v_pk_max_u16 v52, v48, v49\n"
    "s_add_u32 s31, s47, 0xe00\n"
    "buffer_load_dwordx4 v8, s[20:23], s31 offen offset:1024 lds\n"
    "v_pk_max_u16 v53, v50, v51\n"
    "v_pk_min_u16 v54, v48, v49\n"
    "v_pk_min_u16 v55, v50, v51\n"
    "v_pk_max_u16 v52, v52, v54 op_sel:[0,1] op_sel_hi:[1,0]\n"
    "v_pk_max_u16 v53, v53, v55 op_sel:[0,1] op_sel_hi:[1,0]\n"
    "v_pk_min_u16 v52, v52, v52 op_sel:[0,1] op_sel_hi:[1,0]\n"
    "s_add_u32 s31, s47, 0x1c00\n"
    "buffer_load_dwordx4 v8, s[20:23], s31 offen offset:2048 lds\n"
    "v_pk_min_u16 v53, v53, v53 op_sel:[0,1] op_sel_hi:[1,0]\n"
    "v_pk_sub_i16 v48, v48, v52\n"
    "v_pk_sub_i16 v49, v49, v52\n"
    "v_pk_sub_i16 v50, v50, v53\n"
    "v_pk_sub_i16 v51, v51, v53\n"
    "v_pk_ashrrev_i16 v48, 15, v48 op_sel_hi:[0,1]\n"
    "s_add_u32 s31, s47, 0x2a00\n"
    "buffer_load_dwordx4 v8, s[20:23], s31 offen offset:3072 lds\n"
    "v_pk_ashrrev_i16 v49, 15, v49 op_sel_hi:[0,1]\n"
    "v_pk_ashrrev_i16 v50, 15, v50 op_sel_hi:[0,1]\n"
    "v_pk_ashrrev_i16 v51, 15, v51 op_sel_hi:[0,1]\n"
    "v_bitop3_b32 v16, v16, v48, v16 bitop3:0x30\n"
    "v_bitop3_b32 v17, v17, v49, v17 bitop3:0x30\n"
    "v_bitop3_b32 v18, v18, v50, v18 bitop3:0x30\n"
    "s_add_u32 m0, s35, 0x1c000\n"
    "s_nop 0\n"
    "buffer_load_dwordx4 v9, s[12:15], s55 offen lds\n"
    "v_bitop3_b32 v19, v19, v51, v19 bitop3:0x30\n"
    "ds_write_b128 v3, v[16:19] offset:32768\n"
    "s_add_u32 s31, s55, 0x8c00\n"
    "buffer_load_dwordx4 v9, s[12:15], s31 offen offset:1024 lds\n"
    "s_add_u32 s31, s55, 0x11800\n"
    "buffer_load_dwordx4 v9, s[12:15], s31 offen offset:2048 lds\n"
    "s_add_u32 s31, s55, 0x1a400\n"
    "buffer_load_dwordx4 v9, s[12:15], s31 offen offset:3072 lds\n"
    "s_waitcnt vmcnt(8) lgkmcnt(0)\n"
    "s_barrier\n"
    "ds_read_b64_tr_b16 v[16:17], v2 offset:16384\n"
    "ds_read_b64_tr_b16 v[18:19], v2 offset:17408\n"
    "s_add_u32 s48, s47, 0x12000\n"
    "s_cmp_eq_u32 s48, 0xa2000\n"
    "s_cselect_b32 s48, 0, s48\n"
    "s_add_u32 s57, s56, 0x80\n"
    "s_cmp_eq_u32 s57, s34\n"
    "s_cselect_b32 s57, s24, s57\n"
    "s_add_u32 m0, s38, 0xc000\n"
    "s_nop 0\n"
    "buffer_load_dwordx4 v8, s[20:23], s48 offen lds\n"
    "s_waitcnt lgkmcnt(0)\n"
    "v_and_b32 v48, s39, v16\n"
    "v_and_b32 v49, s39, v17\n"
    "v_and_b32 v50, s39, v18\n"
    "v_and_b32 v51, s39, v19\n"
    "v_pk_max_u16 v52, v48, v49\n"
    "s_add_u32 s31, s48, 0xe00\n"
    "buffer_load_dwordx4 v8, s[20:23], s31 offen offset:1024 lds\n"
    "v_pk_max_u16 v53, v50, v51\n"
    "v_pk_min_u16 v54, v48, v49\n"
    "v_pk_min_u16 v55, v50, v51\n"
    "v_pk_max_u16 v52, v52, v54 op_sel:[0,1] op_sel_hi:[1,0]\n"
    "v_pk_max_u16 v53, v53, v55 op_sel:[0,1] op_sel_hi:[1,0]\n"
    "v_pk_min_u16 v52, v52, v52 op_sel:[0,1] op_sel_hi:[1,0]\n"
    "s_add_u32 s31, s48, 0x1c00\n"
    "buffer_load_dwordx4 v8, s[20:23], s31 offen offset:2048 lds\n"
    "v_pk_min_u16 v53, v53, v53 op_sel:[0,1] op_sel_hi:[1,0]\n"
    "v_pk_sub_i16 v48, v48, v52\n"
    "v_pk_sub_i16 v49, v49, v52\n"
    "v_pk_sub_i16 v50, v50, v53\n"
    "v_pk_sub_i16 v51, v51, v53\n"
    "v_pk_ashrrev_i16 v48, 15, v48 op_sel_hi:[0,1]\n"
    "s_add_u32 s31, s48, 0x2a00\n"
    "buffer_load_dwordx4 v8, s[20:23], s31 offen offset:3072 lds\n"
    "v_pk_ashrrev_i16 v49, 15, v49 op_sel_hi:[0,1]\n"
    "v_pk_ashrrev_i16 v50, 15, v50 op_sel_hi:[0,1]\n"
    "v_pk_ashrrev_i16 v51, 15, v51 op_sel_hi:[0,1]\n"
    "v_bitop3_b32 v16, v16, v48, v16 bitop3:0x30\n"
    "v_bitop3_b32 v17, v17, v49, v17 bitop3:0x30\n"
    "v_bitop3_b32 v18, v18, v50, v18 bitop3:0x30\n"
    "s_add_u32 m0, s35, 0x20000\n"
    "s_nop 0\n"
    "buffer_load_dwordx4 v9, s[12:15], s56 offen lds\n"
    "v_bitop3_b32 v19, v19, v51, v19 bitop3:0x30\n"
    "ds_write_b128 v3, v[16:19] offset:36864\n"
    "s_add_u32 s31, s56, 0x8c00\n"
    "buffer_load_dwordx4 v9, s[12:15], s31 offen offset:1024 lds\n"
    "s_add_u32 s31, s56, 0x11800\n"
    "buffer_load_dwordx4 v9, s[12:15], s31 offen offset:2048 lds\n"
    "s_add_u32 s31, s56, 0x1a400\n"
    "buffer_load_dwordx4 v9, s[12:15], s31 offen offset:3072 lds\n"
    "s_waitcnt vmcnt(8) lgkmcnt(0)\n"
    "s_barrier\n"
    "ds_read_b64_tr_b16 v[16:17], v2 offset:32768\n"
    "ds_read_b64_tr_b16 v[18:19], v2 offset:33792\n"
    "s_add_u32 s49, s48, 0x12000\n"
    "s_cmp_eq_u32 s49, 0xa2000\n"
    "s_cselect_b32 s49, 0, s49\n"
    "s_add_u32 s58, s57, 0x80\n"
    "s_cmp_eq_u32 s58, s34\n"
    "s_cselect_b32 s58, s24, s58\n"
    "s_add_u32 m0, s38, 0x0\n"
    "s_nop 0\n"
    "buffer_load_dwordx4 v8, s[20:23], s49 offen lds\n"
    "s_waitcnt lgkmcnt(0)\n"
    "v_and_b32 v48, s39, v16\n"
    "v_and_b32 v49, s39, v17\n"
    "v_and_b32 v50, s39, v18\n"
    "v_and_b32_e64 v51, s39, v19\n"
    "v_pk_max_u16 v52, v48, v49\n"
    "s_add_u32 s31, s49, 0xe00\n"
    "buffer_load_dwordx4 v8, s[20:23], s31 offen offset:1024 lds\n"
    "v_pk_max_u16 v53, v50, v51\n"
    "v_pk_min_u16 v54, v48, v49\n"
    "v_pk_min_u16 v55, v50, v51\n"
    "v_pk_max_u16 v52, v52, v54 op_sel:[0,1] op_sel_hi:[1,0]\n"
    "v_pk_max_u16 v53, v53, v55 op_sel:[0,1] op_sel_hi:[1,0]\n"
    "v_pk_min_u16 v52, v52, v52 op_sel:[0,1] op_sel_hi:[1,0]\n"
    "s_add_u32 s31, s49, 0x1c00\n"
    "buffer_load_dwordx4 v8, s[20:23], s31 offen offset:2048 lds\n"
    "v_pk_min_u16 v53, v53, v53 op_sel:[0,1] op_sel_hi:[1,0]\n"
    "v_pk_sub_i16 v48, v48, v52\n"
    "v_pk_sub_i16 v49, v49, v52\n"
    "v_pk_sub_i16 v50, v50, v53\n"
    "v_pk_sub_i16 v51, v51, v53\n"
    "v_pk_ashrrev_i16 v48, 15, v48 op_sel_hi:[0,1]\n"
    "s_add_u32 s31, s49, 0x2a00\n"
    "buffer_load_dwordx4 v8, s[20:23], s31 offen offset:3072 lds\n"
    "v_pk_ashrrev_i16 v49, 15, v49 op_sel_hi:[0,1]\n"
    "v_pk_ashrrev_i16 v50, 15, v50 op_sel_hi:[0,1]\n"
    "v_pk_ashrrev_i16 v51, 15, v51 op_sel_hi:[0,1]\n"
    "v_bitop3_b32 v16, v16, v48, v16 bitop3:0x30\n"
    "v_bitop3_b32 v17, v17, v49, v17 bitop3:0x30\n"
    "v_bitop3_b32 v18, v18, v50, v18 bitop3:0x30\n"
    "s_add_u32 m0, s35, 0x24000\n"
    "s_nop 0\n"
    "buffer_load_dwordx4 v9, s[12:15], s57 offen lds\n"
    "v_bitop3_b32 v19, v19, v51, v19 bitop3:0x30\n"
    "ds_write_b128 v3, v[16:19] offset:32768\n"
    "s_add_u32 s31, s57, 0x8c00\n"
    "buffer_load_dwordx4 v9, s[12:15], s31 offen offset:1024 lds\n"
    "s_add_u32 s31, s57, 0x11800\n"
    "buffer_load_dwordx4 v9, s[12:15], s31 offen offset:2048 lds\n"
    "s_add_u32 s31, s57, 0x1a400\n"
    "buffer_load_dwordx4 v9, s[12:15], s31 offen offset:3072 lds\n"
    "s_waitcnt vmcnt(8) lgkmcnt(0)\n"
    "s_barrier\n"
    "ds_read_b64_tr_b16 v[16:17], v2 offset:49152\n"
    "ds_read_b64_tr_b16 v[18:19], v2 offset:50176\n"
    "s_add_u32 m0, s35, 0x18000\n"
    "s_nop 0\n"
    "buffer_load_dwordx4 v9, s[12:15], s58 offen lds\n"
    "s_waitcnt lgkmcnt(0)\n"
    "v_and_b32 v48, s39, v16\n"
    "v_and_b32 v49, s39, v17\n"
    "v_and_b32 v50, s39, v18\n"
    "v_and_b32_e64 v51, s39, v19\n"
    "v_pk_max_u16 v52, v48, v49\n"
    "s_add_u32 s31, s58, 0x8c00\n"
    "buffer_load_dwordx4 v9, s[12:15], s31 offen offset:1024 lds\n"
    "v_pk_max_u16 v53, v50, v51\n"
    "v_pk_min_u16 v54, v48, v49\n"
    "v_pk_min_u16 v55, v50, v51\n"
    "v_pk_max_u16 v52, v52, v54 op_sel:[0,1] op_sel_hi:[1,0]\n"
    "v_pk_max_u16 v53, v53, v55 op_sel:[0,1] op_sel_hi:[1,0]\n"
    "v_pk_min_u16 v52, v52, v52 op_sel:[0,1] op_sel_hi:[1,0]\n"
    "s_add_u32 s31, s58, 0x11800\n"
    "buffer_load_dwordx4 v9, s[12:15], s31 offen offset:2048 lds\n"
    "v_pk_min_u16 v53, v53, v53 op_sel:[0,1] op_sel_hi:[1,0]\n"
    "v_pk_sub_i16 v48, v48, v52\n"
    "v_pk_sub_i16 v49, v49, v52\n"
    "v_pk_sub_i16 v50, v50, v53\n"
    "v_pk_sub_i16 v51, v51, v53\n"
    "v_pk_ashrrev_i16 v48, 15, v48 op_sel_hi:[0,1]\n"
    "s_add_u32 s31, s58, 0x1a400\n"
    "buffer_load_dwordx4 v9, s[12:15], s31 offen offset:3072 lds\n"
    "v_pk_ashrrev_i16 v49, 15, v49 op_sel_hi:[0,1]\n"
    "v_pk_ashrrev_i16 v50, 15, v50 op_sel_hi:[0,1]\n"
    "v_pk_ashrrev_i16 v51, 15, v51 op_sel_hi:[0,1]\n"
    "v_bitop3_b32 v16, v16, v48, v16 bitop3:0x30\n"
    "v_bitop3_b32 v17, v17, v49, v17 bitop3:0x30\n"
    "v_bitop3_b32 v18, v18, v50, v18 bitop3:0x30\n"
    "s_add_u32 m0, s35, 0x14000\n"
    "s_add_u32 s31, s50, 0x24000\n"
    "buffer_load_dwordx4 v9, s[12:15], s31 offen lds\n"
    "v_bitop3_b32 v19, v19, v51, v19 bitop3:0x30\n"
    "ds_write_b128 v3, v[16:19] offset:36864\n"
    "s_add_u32 s31, s51, 0x23c00\n"
    "buffer_load_dwordx4 v9, s[12:15], s31 offen offset:1024 lds\n"
    "s_add_u32 s31, s52, 0x23800\n"
    "buffer_load_dwordx4 v9, s[12:15], s31 offen offset:2048 lds\n"
    "s_add_u32 s31, s53, 0x23400\n"
    "buffer_load_dwordx4 v9, s[12:15], s31 offen offset:3072 lds\n"
    "s_waitcnt vmcnt(8) lgkmcnt(0)\n"
    "s_barrier\n"
    "ds_read_b64_tr_b16 v[16:17], v2 offset:0\n"
    "ds_read_b64_tr_b16 v[18:19], v2 offset:1024\n"
    "s_add_u32 m0, s35, 0x1c000\n"
    "s_add_u32 s31, s54, 0x24000\n"
    "buffer_load_dwordx4 v9, s[12:15], s31 offen lds\n"
    "s_waitcnt lgkmcnt(0)\n"
    "v_and_b32 v48, s39, v16\n"
    "v_and_b32 v49, s39, v17\n"
    "v_and_b32 v50, s39, v18\n"
    "v_and_b32_e64 v51, s39, v19\n"
    "v_pk_max_u16 v52, v48, v49\n"
    "s_add_u32 s31, s55, 0x23c00\n"
    "buffer_load_dwordx4 v9, s[12:15], s31 offen offset:1024 lds\n"
    "v_pk_max_u16 v53, v50, v51\n"
    "v_pk_min_u16 v54, v48, v49\n"
    "v_pk_min_u16 v55, v50, v51\n"
    "v_pk_max_u16 v52, v52, v54 op_sel:[0,1] op_sel_hi:[1,0]\n"
    "v_pk_max_u16 v53, v53, v55 op_sel:[0,1] op_sel_hi:[1,0]\n"
    "v_pk_min_u16 v52, v52, v52 op_sel:[0,1] op_sel_hi:[1,0]\n"
    "s_add_u32 s31, s56, 0x23800\n"
    "buffer_load_dwordx4 v9, s[12:15], s31 offen offset:2048 lds\n"
    "v_pk_min_u16 v53, v53, v53 op_sel:[0,1] op_sel_hi:[1,0]\n"
    "v_pk_sub_i16 v48, v48, v52\n"
    "v_pk_sub_i16 v49, v49, v52\n"
    "v_pk_sub_i16 v50, v50, v53\n"
    "v_pk_sub_i16 v51, v51, v53\n"
    "v_pk_ashrrev_i16 v48, 15, v48 op_sel_hi:[0,1]\n"
    "s_add_u32 s31, s57, 0x23400\n"
    "buffer_load_dwordx4 v9, s[12:15], s31 offen offset:3072 lds\n"
    "v_pk_ashrrev_i16 v49, 15, v49 op_sel_hi:[0,1]\n"
    "v_pk_ashrrev_i16 v50, 15, v50 op_sel_hi:[0,1]\n"
    "v_pk_ashrrev_i16 v51, 15, v51 op_sel_hi:[0,1]\n"
    "v_bitop3_b32 v16, v16, v48, v16 bitop3:0x30\n"
    "v_bitop3_b32 v17, v17, v49, v17 bitop3:0x30\n"
    "v_bitop3_b32 v18, v18, v50, v18 bitop3:0x30\n"
    "v_bitop3_b32 v19, v19, v51, v19 bitop3:0x30\n"
    "ds_write_b128 v3, v[16:19] offset:32768\n"
    "s_waitcnt vmcnt(8) lgkmcnt(0)\n"
    "s_barrier\n"
    "s_add_u32 m0, s35, 0x20000\n"
    "s_add_u32 s31, s58, 0x24000\n"
    "buffer_load_dwordx4 v9, s[12:15], s31 offen lds\n"
    "v_mov_b32 v12, 0\n"
    "v_mov_b32 v13, 0\n"
    "v_mov_b32 v14, 0\n"
    "v_mov_b32 v15, 0\n"
    "ds_write_b128 v6, v[12:15]\n"
    "s_waitcnt vmcnt(0) lgkmcnt(0)\n"
    "s_barrier\n"
    ".Lpoll0_%=:\n"
    "s_sleep 1\n"
    "ds_read_b32 v7, v6 offset:0\n"
    "s_waitcnt lgkmcnt(0)\n"
    "v_cmp_eq_u32 vcc, 0, v7\n"
    "s_cbranch_vccnz .Lpoll0_%=\n"
    "ds_read_b128 v[16:19], v4 offset:0\n"
    "ds_read_b128 v[20:23], v4 offset:1024\n"
    "s_add_u32 s28, s36, 0x0\n"
    "s_waitcnt lgkmcnt(1)\n"
    "buffer_store_dwordx4 v[16:19], v5, s[16:19], s28 offen nt\n"
    "s_add_u32 s28, s36, 0x4800\n"
    "s_waitcnt lgkmcnt(0)\n"
    "buffer_store_dwordx4 v[20:23], v5, s[16:19], s28 offen nt\n"
    ".Lpoll1_%=:\n"
    "s_sleep 1\n"
    "ds_read_b32 v7, v6 offset:4\n"
    "s_waitcnt lgkmcnt(0)\n"
    "v_cmp_eq_u32_e64 vcc, 0, v7\n"
    "s_cbranch_vccnz .Lpoll1_%=\n"
    "ds_read_b128 v[24:27], v4 offset:8192\n"
    "ds_read_b128 v[28:31], v4 offset:9216\n"
    "s_add_u32 s28, s36, 0x9000\n"
    "s_waitcnt lgkmcnt(1)\n"
    "buffer_store_dwordx4 v[24:27], v5, s[16:19], s28 offen nt\n"
    "s_add_u32 s28, s36, 0xd800\n"
    "s_waitcnt lgkmcnt(0)\n"
    "buffer_store_dwordx4 v[28:31], v5, s[16:19], s28 offen nt\n"
    ".Lpoll2_%=:\n"
    "s_sleep 1\n"
    "ds_read_b32 v7, v6 offset:8\n"
    "s_waitcnt lgkmcnt(0)\n"
    "v_cmp_eq_u32 vcc, 0, v7\n"
    "s_cbranch_vccnz .Lpoll2_%=\n"
    "ds_read_b128 v[32:35], v4 offset:16384\n"
    "ds_read_b128 v[36:39], v4 offset:17408\n"
    "s_add_u32 s28, s36, 0x12000\n"
    "s_waitcnt lgkmcnt(1)\n"
    "buffer_store_dwordx4 v[32:35], v5, s[16:19], s28 offen nt\n"
    "s_add_u32 s28, s36, 0x16800\n"
    "s_waitcnt lgkmcnt(0)\n"
    "buffer_store_dwordx4 v[36:39], v5, s[16:19], s28 offen nt\n"
    ".Lpoll3_%=:\n"
    "s_sleep 1\n"
    "ds_read_b32 v7, v6 offset:12\n"
    "s_waitcnt lgkmcnt(0)\n"
    "v_cmp_eq_u32 vcc, 0, v7\n"
    "s_cbranch_vccnz .Lpoll3_%=\n"
    "ds_read_b128 v[40:43], v4 offset:24576\n"
    "ds_read_b128 v[44:47], v4 offset:25600\n"
    "s_add_u32 s28, s36, 0x1b000\n"
    "s_waitcnt lgkmcnt(1)\n"
    "buffer_store_dwordx4 v[40:43], v5, s[16:19], s28 offen nt\n"
    "s_add_u32 s28, s36, 0x1f800\n"
    "s_waitcnt lgkmcnt(0)\n"
    "buffer_store_dwordx4 v[44:47], v5, s[16:19], s28 offen nt\n"
    "s_branch .Lend%=\n"
    ".p2align 3\n"
    ".Lhelp_b%=:\n"
    "v_lshrrev_b32 v10, 3, v1\n"
    "v_lshlrev_b32 v11, 1, v10\n"
    "v_and_b32 v11, 4, v11\n"
    "v_xor_b32 v11, v11, v1\n"
    "v_and_b32 v11, 7, v11\n"
    "v_lshlrev_b32 v11, 4, v11\n"
    "v_add_u32 v11, s25, v11\n"
    "s_lshl_b32 s28, s27, 4\n"
    "v_add_u32_e64 v10, s28, v10\n"
    "v_mad_u32_u24 v8, v10, s29, v11\n"
    "s_lshl_b32 s38, s27, 11\n"
    "s_add_u32 s38, s38, %3\n"
    "s_mul_i32 s35, s27, 0x1800\n"
    "s_add_u32 m0, s38, 0x0\n"
    "s_nop 0\n"
    "buffer_load_dwordx4 v8, s[20:23], s41 offen lds\n"
    "s_add_u32 s31, s41, 0x2000\n"
    "buffer_load_dwordx4 v8, s[20:23], s31 offen offset:1024 lds\n"
    "s_add_u32 s35, s35, %3\n"
    "s_add_u32 s42, s41, 0x12000\n"
    "s_cmp_eq_u32 s42, 0xa2000\n"
    "s_cselect_b32 s42, 0, s42\n"
    "s_add_u32 s51, s50, 0x80\n"
    "s_cmp_eq_u32 s51, s34\n"
    "s_cselect_b32 s51, s24, s51\n"
    "s_add_u32 m0, s38, 0x2000\n"
    "s_nop 0\n"
    "buffer_load_dwordx4 v8, s[20:23], s42 offen lds\n"
    "s_add_u32 s31, s42, 0x2000\n"
    "buffer_load_dwordx4 v8, s[20:23], s31 offen offset:1024 lds\n"
    "s_add_u32 m0, s35, 0xa000\n"
    "s_nop 0\n"
    "buffer_load_dwordx4 v9, s[12:15], s50 offen lds\n"
    "s_add_u32 s31, s50, 0x8c00\n"
    "buffer_load_dwordx4 v9, s[12:15], s31 offen offset:1024 lds\n"
    "s_add_u32 s31, s50, 0x11800\n"
    "buffer_load_dwordx4 v9, s[12:15], s31 offen offset:2048 lds\n"
    "s_add_u32 s31, s50, 0x1a400\n"
    "buffer_load_dwordx4 v9, s[12:15], s31 offen offset:3072 lds\n"
    "s_add_u32 m0, s35, 0xb000\n"
    "s_add_u32 s31, s50, 0x24000\n"
    "buffer_load_dwordx4 v9, s[12:15], s31 offen lds\n"
    "s_add_u32 s31, s50, 0x2cc00\n"
    "buffer_load_dwordx4 v9, s[12:15], s31 offen offset:1024 lds\n"
    "v_lshrrev_b32 v10, 3, v1\n"
    "v_xor_b32 v11, s27, v10\n"
    "v_and_b32_e64 v11, 1, v11\n"
    "v_bfe_u32 v10, v1, 2, 2\n"
    "v_lshlrev_b32_e64 v11, 6, v11\n"
    "v_lshl_add_u32 v11, v10, 7, v11\n"
    "v_lshrrev_b32_e64 v10, 5, v1\n"
    "v_lshl_add_u32 v11, v10, 10, v11\n"
    "v_and_b32_e64 v10, 1, v1\n"
    "v_lshl_add_u32 v11, v10, 5, v11\n"
    "v_and_b32 v10, 16, v1\n"
    "v_add_u32 v11, v11, v10\n"
    "v_bfe_u32 v10, v1, 1, 1\n"
    "v_lshl_add_u32 v11, v10, 3, v11\n"
    "v_add_u32_e64 v2, %3, v11\n"
    "s_mov_b32 s39, 0x7fff7fff\n"
    "s_mul_i32 s28, s27, 1024\n"
    "s_add_u32 s28, s28, %3\n"
    "s_add_u32 s28, s28, 0x8000\n"
    "v_lshlrev_b32 v3, 4, v1\n"
    "v_add_u32_e64 v3, s28, v3\n"
    "s_add_u32 s43, s42, 0x12000\n"
    "s_cmp_eq_u32 s43, 0xa2000\n"
    "s_cselect_b32 s43, 0, s43\n"
    "s_add_u32 s52, s51, 0x80\n"
    "s_cmp_eq_u32 s52, s34\n"
    "s_cselect_b32 s52, s24, s52\n"
    "s_add_u32 m0, s38, 0x4000\n"
    "s_nop 0\n"
    "buffer_load_dwordx4 v8, s[20:23], s43 offen lds\n"
    "s_add_u32 s31, s43, 0x2000\n"
    "buffer_load_dwordx4 v8, s[20:23], s31 offen offset:1024 lds\n"
    "s_add_u32 m0, s35, 0x10000\n"
    "s_nop 0\n"
    "buffer_load_dwordx4 v9, s[12:15], s51 offen lds\n"
    "s_add_u32 s31, s51, 0x8c00\n"
    "buffer_load_dwordx4 v9, s[12:15], s31 offen offset:1024 lds\n"
    "s_add_u32 s31, s51, 0x11800\n"
    "buffer_load_dwordx4 v9, s[12:15], s31 offen offset:2048 lds\n"
    "s_add_u32 s31, s51, 0x1a400\n"
    "buffer_load_dwordx4 v9, s[12:15], s31 offen offset:3072 lds\n"
    "s_add_u32 m0, s35, 0x11000\n"
    "s_add_u32 s31, s51, 0x24000\n"
    "buffer_load_dwordx4 v9, s[12:15], s31 offen lds\n"
    "s_add_u32 s31, s51, 0x2cc00\n"
    "buffer_load_dwordx4 v9, s[12:15], s31 offen offset:1024 lds\n"
    "s_waitcnt vmcnt(16)\n"
    "s_barrier\n"
    "ds_read_b64_tr_b16 v[16:17], v2 offset:0\n"
    "ds_read_b64_tr_b16 v[18:19], v2 offset:512\n"
    "s_waitcnt lgkmcnt(0)\n"
    "v_and_b32 v48, s39, v16\n"
    "v_and_b32 v49, s39, v17\n"
    "v_and_b32 v50, s39, v18\n"
    "v_and_b32 v51, s39, v19\n"
    "v_pk_max_u16 v52, v48, v49\n"
    "v_pk_max_u16 v53, v50, v51\n"
    "v_pk_min_u16 v54, v48, v49\n"
    "v_pk_min_u16 v55, v50, v51\n"
    "v_pk_max_u16 v52, v52, v54 op_sel:[0,1] op_sel_hi:[1,0]\n"
    "v_pk_max_u16 v53, v53, v55 op_sel:[0,1] op_sel_hi:[1,0]\n"
    "v_pk_min_u16 v52, v52, v52 op_sel:[0,1] op_sel_hi:[1,0]\n"
    "v_pk_min_u16 v53, v53, v53 op_sel:[0,1] op_sel_hi:[1,0]\n"
    "v_pk_sub_i16 v48, v48, v52\n"
    "v_pk_sub_i16 v49, v49, v52\n"
    "v_pk_sub_i16 v50, v50, v53\n"
    "v_pk_sub_i16 v51, v51, v53\n"
    "v_pk_ashrrev_i16 v48, 15, v48 op_sel_hi:[0,1]\n"
    "v_pk_ashrrev_i16 v49, 15, v49 op_sel_hi:[0,1]\n"
    "v_pk_ashrrev_i16 v50, 15, v50 op_sel_hi:[0,1]\n"
    "v_pk_ashrrev_i16 v51, 15, v51 op_sel_hi:[0,1]\n"
    "v_bitop3_b32 v16, v16, v48, v16 bitop3:0x30\n"
    "v_bitop3_b32 v17, v17, v49, v17 bitop3:0x30\n"
    "v_bitop3_b32 v18, v18, v50, v18 bitop3:0x30\n"
    "v_bitop3_b32 v19, v19, v51, v19 bitop3:0x30\n"
    "ds_write_b128 v3, v[16:19] offset:0\n"
    "s_waitcnt vmcnt(8) lgkmcnt(0)\n"
    "s_barrier\n"
    "ds_read_b64_tr_b16 v[16:17], v2 offset:8192\n"
    "ds_read_b64_tr_b16 v[18:19], v2 offset:8704\n"
    "s_add_u32 s44, s43, 0x12000\n"
    "s_cmp_eq_u32 s44, 0xa2000\n"
    "s_cselect_b32 s44, 0, s44\n"
    "s_add_u32 s53, s52, 0x80\n"
    "s_cmp_eq_u32 s53, s34\n"
    "s_cselect_b32 s53, s24, s53\n"
    "s_add_u32 m0, s38, 0x6000\n"
    "s_nop 0\n"
    "buffer_load_dwordx4 v8, s[20:23], s44 offen lds\n"
    "s_waitcnt lgkmcnt(0)\n"
    "v_and_b32 v48, s39, v16\n"
    "v_and_b32 v49, s39, v17\n"
    "v_and_b32 v50, s39, v18\n"
    "v_and_b32_e64 v51, s39, v19\n"
    "v_pk_max_u16 v52, v48, v49\n"
    "s_add_u32 s31, s44, 0x2000\n"
    "buffer_load_dwordx4 v8, s[20:23], s31 offen offset:1024 lds\n"
    "v_pk_max_u16 v53, v50, v51\n"
    "v_pk_min_u16 v54, v48, v49\n"
    "v_pk_min_u16 v55, v50, v51\n"
    "v_pk_max_u16 v52, v52, v54 op_sel:[0,1] op_sel_hi:[1,0]\n"
    "v_pk_max_u16 v53, v53, v55 op_sel:[0,1] op_sel_hi:[1,0]\n"
    "v_pk_min_u16 v52, v52, v52 op_sel:[0,1] op_sel_hi:[1,0]\n"
    "s_add_u32 m0, s35, 0x16000\n"
    "s_nop 0\n"
    "buffer_load_dwordx4 v9, s[12:15], s52 offen lds\n"
    "v_pk_min_u16 v53, v53, v53 op_sel:[0,1] op_sel_hi:[1,0]\n"
    "v_pk_sub_i16 v48, v48, v52\n"
    "v_pk_sub_i16 v49, v49, v52\n"
    "v_pk_sub_i16 v50, v50, v53\n"
    "v_pk_sub_i16 v51, v51, v53\n"
    "v_pk_ashrrev_i16 v48, 15, v48 op_sel_hi:[0,1]\n"
    "s_add_u32 s31, s52, 0x8c00\n"
    "buffer_load_dwordx4 v9, s[12:15], s31 offen offset:1024 lds\n"
    "v_pk_ashrrev_i16 v49, 15, v49 op_sel_hi:[0,1]\n"
    "v_pk_ashrrev_i16 v50, 15, v50 op_sel_hi:[0,1]\n"
    "v_pk_ashrrev_i16 v51, 15, v51 op_sel_hi:[0,1]\n"
    "v_bitop3_b32 v16, v16, v48, v16 bitop3:0x30\n"
    "v_bitop3_b32 v17, v17, v49, v17 bitop3:0x30\n"
    "v_bitop3_b32 v18, v18, v50, v18 bitop3:0x30\n"
    "s_add_u32 s31, s52, 0x11800\n"
    "buffer_load_dwordx4 v9, s[12:15], s31 offen offset:2048 lds\n"
    "v_bitop3_b32 v19, v19, v51, v19 bitop3:0x30\n"
    "ds_write_b128 v3, v[16:19] offset:4096\n"
    "s_add_u32 s31, s52, 0x1a400\n"
    "buffer_load_dwordx4 v9, s[12:15], s31 offen offset:3072 lds\n"
    "s_add_u32 m0, s35, 0x17000\n"
    "s_add_u32 s31, s52, 0x24000\n"
    "buffer_load_dwordx4 v9, s[12:15], s31 offen lds\n"
    "s_add_u32 s31, s52, 0x2cc00\n"
    "buffer_load_dwordx4 v9, s[12:15], s31 offen offset:1024 lds\n"
    "s_waitcnt vmcnt(8) lgkmcnt(0)\n"
    "s_barrier\n"
    "ds_read_b64_tr_b16 v[16:17], v2 offset:16384\n"
    "ds_read_b64_tr_b16 v[18:19], v2 offset:16896\n"
    "s_add_u32 s45, s44, 0x12000\n"
    "s_cmp_eq_u32 s45, 0xa2000\n"
    "s_cselect_b32 s45, 0, s45\n"
    "s_add_u32 s54, s53, 0x80\n"
    "s_cmp_eq_u32 s54, s34\n"
    "s_cselect_b32 s54, s24, s54\n"
    "s_add_u32 m0, s38, 0x0\n"
    "s_nop 0\n"
    "buffer_load_dwordx4 v8, s[20:23], s45 offen lds\n"
    "s_waitcnt lgkmcnt(0)\n"
    "v_and_b32 v48, s39, v16\n"
    "v_and_b32 v49, s39, v17\n"
    "v_and_b32 v50, s39, v18\n"
    "v_and_b32_e64 v51, s39, v19\n"
    "v_pk_max_u16 v52, v48, v49\n"
    "s_add_u32 s31, s45, 0x2000\n"
    "buffer_load_dwordx4 v8, s[20:23], s31 offen offset:1024 lds\n"
    "v_pk_max_u16 v53, v50, v51\n"
    "v_pk_min_u16 v54, v48, v49\n"
    "v_pk_min_u16 v55, v50, v51\n"
    "v_pk_max_u16 v52, v52, v54 op_sel:[0,1] op_sel_hi:[1,0]\n"
    "v_pk_max_u16 v53, v53, v55 op_sel:[0,1] op_sel_hi:[1,0]\n"
    "v_pk_min_u16 v52, v52, v52 op_sel:[0,1] op_sel_hi:[1,0]\n"
    "s_add_u32 m0, s35, 0x1c000\n"
    "s_nop 0\n"
    "buffer_load_dwordx4 v9, s[12:15], s53 offen lds\n"
    "v_pk_min_u16 v53, v53, v53 op_sel:[0,1] op_sel_hi:[1,0]\n"
    "v_pk_sub_i16 v48, v48, v52\n"
    "v_pk_sub_i16 v49, v49, v52\n"
    "v_pk_sub_i16 v50, v50, v53\n"
    "v_pk_sub_i16 v51, v51, v53\n"
    "v_pk_ashrrev_i16 v48, 15, v48 op_sel_hi:[0,1]\n"
    "s_add_u32 s31, s53, 0x8c00\n"
    "buffer_load_dwordx4 v9, s[12:15], s31 offen offset:1024 lds\n"
    "v_pk_ashrrev_i16 v49, 15, v49 op_sel_hi:[0,1]\n"
    "v_pk_ashrrev_i16 v50, 15, v50 op_sel_hi:[0,1]\n"
    "v_pk_ashrrev_i16 v51, 15, v51 op_sel_hi:[0,1]\n"
    "v_bitop3_b32 v16, v16, v48, v16 bitop3:0x30\n"
    "v_bitop3_b32 v17, v17, v49, v17 bitop3:0x30\n"
    "v_bitop3_b32 v18, v18, v50, v18 bitop3:0x30\n"
    "s_add_u32 s31, s53, 0x11800\n"
    "buffer_load_dwordx4 v9, s[12:15], s31 offen offset:2048 lds\n"
    "v_bitop3_b32 v19, v19, v51, v19 bitop3:0x30\n"
    "ds_write_b128 v3, v[16:19] offset:0\n"
    "s_add_u32 s31, s53, 0x1a400\n"
    "buffer_load_dwordx4 v9, s[12:15], s31 offen offset:3072 lds\n"
    "s_add_u32 m0, s35, 0x1d000\n"
    "s_add_u32 s31, s53, 0x24000\n"
    "buffer_load_dwordx4 v9, s[12:15], s31 offen lds\n"
    "s_add_u32 s31, s53, 0x2cc00\n"
    "buffer_load_dwordx4 v9, s[12:15], s31 offen offset:1024 lds\n"
    "s_waitcnt vmcnt(8) lgkmcnt(0)\n"
    "s_barrier\n"
    "ds_read_b64_tr_b16 v[16:17], v2 offset:24576\n"
    "ds_read_b64_tr_b16 v[18:19], v2 offset:25088\n"
    "s_add_u32 s46, s45, 0x12000\n"
    "s_cmp_eq_u32 s46, 0xa2000\n"
    "s_cselect_b32 s46, 0, s46\n"
    "s_add_u32 s55, s54, 0x80\n"
    "s_cmp_eq_u32 s55, s34\n"
    "s_cselect_b32 s55, s24, s55\n"
    "s_add_u32 m0, s38, 0x2000\n"
    "s_nop 0\n"
    "buffer_load_dwordx4 v8, s[20:23], s46 offen lds\n"
    "s_waitcnt lgkmcnt(0)\n"
    "v_and_b32 v48, s39, v16\n"
    "v_and_b32 v49, s39, v17\n"
    "v_and_b32 v50, s39, v18\n"
    "v_and_b32 v51, s39, v19\n"
    "v_pk_max_u16 v52, v48, v49\n"
    "s_add_u32 s31, s46, 0x2000\n"
    "buffer_load_dwordx4 v8, s[20:23], s31 offen offset:1024 lds\n"
    "v_pk_max_u16 v53, v50, v51\n"
    "v_pk_min_u16 v54, v48, v49\n"
    "v_pk_min_u16 v55, v50, v51\n"
    "v_pk_max_u16 v52, v52, v54 op_sel:[0,1] op_sel_hi:[1,0]\n"
    "v_pk_max_u16 v53, v53, v55 op_sel:[0,1] op_sel_hi:[1,0]\n"
    "v_pk_min_u16 v52, v52, v52 op_sel:[0,1] op_sel_hi:[1,0]\n"
    "s_add_u32 m0, s35, 0x22000\n"
    "s_nop 0\n"
    "buffer_load_dwordx4 v9, s[12:15], s54 offen lds\n"
    "v_pk_min_u16 v53, v53, v53 op_sel:[0,1] op_sel_hi:[1,0]\n"
    "v_pk_sub_i16 v48, v48, v52\n"
    "v_pk_sub_i16 v49, v49, v52\n"
    "v_pk_sub_i16 v50, v50, v53\n"
    "v_pk_sub_i16 v51, v51, v53\n"
    "v_pk_ashrrev_i16 v48, 15, v48 op_sel_hi:[0,1]\n"
    "s_add_u32 s31, s54, 0x8c00\n"
    "buffer_load_dwordx4 v9, s[12:15], s31 offen offset:1024 lds\n"
    "v_pk_ashrrev_i16 v49, 15, v49 op_sel_hi:[0,1]\n"
    "v_pk_ashrrev_i16 v50, 15, v50 op_sel_hi:[0,1]\n"
    "v_pk_ashrrev_i16 v51, 15, v51 op_sel_hi:[0,1]\n"
    "v_bitop3_b32 v16, v16, v48, v16 bitop3:0x30\n"
    "v_bitop3_b32 v17, v17, v49, v17 bitop3:0x30\n"
    "v_bitop3_b32 v18, v18, v50, v18 bitop3:0x30\n"
    "s_add_u32 s31, s54, 0x11800\n"
    "buffer_load_dwordx4 v9, s[12:15], s31 offen offset:2048 lds\n"
    "v_bitop3_b32 v19, v19, v51, v19 bitop3:0x30\n"
    "ds_write_b128 v3, v[16:19] offset:4096\n"
    "s_add_u32 s31, s54, 0x1a400\n"
    "buffer_load_dwordx4 v9, s[12:15], s31 offen offset:3072 lds\n"
    "s_add_u32 m0, s35, 0x23000\n"
    "s_add_u32 s31, s54, 0x24000\n"
    "buffer_load_dwordx4 v9, s[12:15], s31 offen lds\n"
    "s_add_u32 s31, s54, 0x2cc00\n"
    "buffer_load_dwordx4 v9, s[12:15], s31 offen offset:1024 lds\n"
    "s_waitcnt vmcnt(8) lgkmcnt(0)\n"
    "s_barrier\n"
    "ds_read_b64_tr_b16 v[16:17], v2 offset:0\n"
    "ds_read_b64_tr_b16 v[18:19], v2 offset:512\n"
    "s_add_u32 s47, s46, 0x12000\n"
    "s_cmp_eq_u32 s47, 0xa2000\n"
    "s_cselect_b32 s47, 0, s47\n"
    "s_add_u32 s56, s55, 0x80\n"
    "s_cmp_eq_u32 s56, s34\n"
    "s_cselect_b32 s56, s24, s56\n"
    "s_add_u32 m0, s38, 0x4000\n"
    "s_nop 0\n"
    "buffer_load_dwordx4 v8, s[20:23], s47 offen lds\n"
    "s_waitcnt lgkmcnt(0)\n"
    "v_and_b32 v48, s39, v16\n"
    "v_and_b32 v49, s39, v17\n"
    "v_and_b32 v50, s39, v18\n"
    "v_and_b32 v51, s39, v19\n"
    "v_pk_max_u16 v52, v48, v49\n"
    "s_add_u32 s31, s47, 0x2000\n"
    "buffer_load_dwordx4 v8, s[20:23], s31 offen offset:1024 lds\n"
    "v_pk_max_u16 v53, v50, v51\n"
    "v_pk_min_u16 v54, v48, v49\n"
    "v_pk_min_u16 v55, v50, v51\n"
    "v_pk_max_u16 v52, v52, v54 op_sel:[0,1] op_sel_hi:[1,0]\n"
    "v_pk_max_u16 v53, v53, v55 op_sel:[0,1] op_sel_hi:[1,0]\n"
    "v_pk_min_u16 v52, v52, v52 op_sel:[0,1] op_sel_hi:[1,0]\n"
    "s_add_u32 m0, s35, 0xa000\n"
    "s_nop 0\n"
    "buffer_load_dwordx4 v9, s[12:15], s55 offen lds\n"
    "v_pk_min_u16 v53, v53, v53 op_sel:[0,1] op_sel_hi:[1,0]\n"
    "v_pk_sub_i16 v48, v48, v52\n"
    "v_pk_sub_i16 v49, v49, v52\n"
    "v_pk_sub_i16 v50, v50, v53\n"
    "v_pk_sub_i16 v51, v51, v53\n"
    "v_pk_ashrrev_i16 v48, 15, v48 op_sel_hi:[0,1]\n"
    "s_add_u32 s31, s55, 0x8c00\n"
    "buffer_load_dwordx4 v9, s[12:15], s31 offen offset:1024 lds\n"
    "v_pk_ashrrev_i16 v49, 15, v49 op_sel_hi:[0,1]\n"
    "v_pk_ashrrev_i16 v50, 15, v50 op_sel_hi:[0,1]\n"
    "v_pk_ashrrev_i16 v51, 15, v51 op_sel_hi:[0,1]\n"
    "v_bitop3_b32 v16, v16, v48, v16 bitop3:0x30\n"
    "v_bitop3_b32 v17, v17, v49, v17 bitop3:0x30\n"
    "v_bitop3_b32 v18, v18, v50, v18 bitop3:0x30\n"
    "s_add_u32 s31, s55, 0x11800\n"
    "buffer_load_dwordx4 v9, s[12:15], s31 offen offset:2048 lds\n"
    "v_bitop3_b32 v19, v19, v51, v19 bitop3:0x30\n"
    "ds_write_b128 v3, v[16:19] offset:0\n"
    "s_add_u32 s31, s55, 0x1a400\n"
    "buffer_load_dwordx4 v9, s[12:15], s31 offen offset:3072 lds\n"
    "s_add_u32 m0, s35, 0xb000\n"
    "s_add_u32 s31, s55, 0x24000\n"
    "buffer_load_dwordx4 v9, s[12:15], s31 offen lds\n"
    "s_add_u32 s31, s55, 0x2cc00\n"
    "buffer_load_dwordx4 v9, s[12:15], s31 offen offset:1024 lds\n"
    "s_waitcnt vmcnt(8) lgkmcnt(0)\n"
    "s_barrier\n"
    "ds_read_b64_tr_b16 v[16:17], v2 offset:8192\n"
    "ds_read_b64_tr_b16 v[18:19], v2 offset:8704\n"
    "s_add_u32 s48, s47, 0x12000\n"
    "s_cmp_eq_u32 s48, 0xa2000\n"
    "s_cselect_b32 s48, 0, s48\n"
    "s_add_u32 s57, s56, 0x80\n"
    "s_cmp_eq_u32 s57, s34\n"
    "s_cselect_b32 s57, s24, s57\n"
    "s_add_u32 m0, s38, 0x6000\n"
    "s_nop 0\n"
    "buffer_load_dwordx4 v8, s[20:23], s48 offen lds\n"
    "s_waitcnt lgkmcnt(0)\n"
    "v_and_b32 v48, s39, v16\n"
    "v_and_b32 v49, s39, v17\n"
    "v_and_b32 v50, s39, v18\n"
    "v_and_b32 v51, s39, v19\n"
    "v_pk_max_u16 v52, v48, v49\n"
    "s_add_u32 s31, s48, 0x2000\n"
    "buffer_load_dwordx4 v8, s[20:23], s31 offen offset:1024 lds\n"
    "v_pk_max_u16 v53, v50, v51\n"
    "v_pk_min_u16 v54, v48, v49\n"
    "v_pk_min_u16 v55, v50, v51\n"
    "v_pk_max_u16 v52, v52, v54 op_sel:[0,1] op_sel_hi:[1,0]\n"
    "v_pk_max_u16 v53, v53, v55 op_sel:[0,1] op_sel_hi:[1,0]\n"
    "v_pk_min_u16 v52, v52, v52 op_sel:[0,1] op_sel_hi:[1,0]\n"
    "s_add_u32 m0, s35, 0x10000\n"
    "s_nop 0\n"
    "buffer_load_dwordx4 v9, s[12:15], s56 offen lds\n"
    "v_pk_min_u16 v53, v53, v53 op_sel:[0,1] op_sel_hi:[1,0]\n"
    "v_pk_sub_i16 v48, v48, v52\n"
    "v_pk_sub_i16 v49, v49, v52\n"
    "v_pk_sub_i16 v50, v50, v53\n"
    "v_pk_sub_i16 v51, v51, v53\n"
    "v_pk_ashrrev_i16 v48, 15, v48 op_sel_hi:[0,1]\n"
    "s_add_u32 s31, s56, 0x8c00\n"
    "buffer_load_dwordx4 v9, s[12:15], s31 offen offset:1024 lds\n"
    "v_pk_ashrrev_i16 v49, 15, v49 op_sel_hi:[0,1]\n"
    "v_pk_ashrrev_i16 v50, 15, v50 op_sel_hi:[0,1]\n"
    "v_pk_ashrrev_i16 v51, 15, v51 op_sel_hi:[0,1]\n"
    "v_bitop3_b32 v16, v16, v48, v16 bitop3:0x30\n"
    "v_bitop3_b32 v17, v17, v49, v17 bitop3:0x30\n"
    "v_bitop3_b32 v18, v18, v50, v18 bitop3:0x30\n"
    "s_add_u32 s31, s56, 0x11800\n"
    "buffer_load_dwordx4 v9, s[12:15], s31 offen offset:2048 lds\n"
    "v_bitop3_b32 v19, v19, v51, v19 bitop3:0x30\n"
    "ds_write_b128 v3, v[16:19] offset:4096\n"
    "s_add_u32 s31, s56, 0x1a400\n"
    "buffer_load_dwordx4 v9, s[12:15], s31 offen offset:3072 lds\n"
    "s_add_u32 m0, s35, 0x11000\n"
    "s_add_u32 s31, s56, 0x24000\n"
    "buffer_load_dwordx4 v9, s[12:15], s31 offen lds\n"
    "s_add_u32 s31, s56, 0x2cc00\n"
    "buffer_load_dwordx4 v9, s[12:15], s31 offen offset:1024 lds\n"
    "s_waitcnt vmcnt(8) lgkmcnt(0)\n"
    "s_barrier\n"
    "ds_read_b64_tr_b16 v[16:17], v2 offset:16384\n"
    "ds_read_b64_tr_b16 v[18:19], v2 offset:16896\n"
    "s_add_u32 s49, s48, 0x12000\n"
    "s_cmp_eq_u32 s49, 0xa2000\n"
    "s_cselect_b32 s49, 0, s49\n"
    "s_add_u32 s58, s57, 0x80\n"
    "s_cmp_eq_u32 s58, s34\n"
    "s_cselect_b32 s58, s24, s58\n"
    "s_add_u32 m0, s38, 0x0\n"
    "s_nop 0\n"
    "buffer_load_dwordx4 v8, s[20:23], s49 offen lds\n"
    "s_waitcnt lgkmcnt(0)\n"
    "v_and_b32 v48, s39, v16\n"
    "v_and_b32 v49, s39, v17\n"
    "v_and_b32 v50, s39, v18\n"
    "v_and_b32_e64 v51, s39, v19\n"
    "v_pk_max_u16 v52, v48, v49\n"
    "s_add_u32 s31, s49, 0x2000\n"
    "buffer_load_dwordx4 v8, s[20:23], s31 offen offset:1024 lds\n"
    "v_pk_max_u16 v53, v50, v51\n"
    "v_pk_min_u16 v54, v48, v49\n"
    "v_pk_min_u16 v55, v50, v51\n"
    "v_pk_max_u16 v52, v52, v54 op_sel:[0,1] op_sel_hi:[1,0]\n"
    "v_pk_max_u16 v53, v53, v55 op_sel:[0,1] op_sel_hi:[1,0]\n"
    "v_pk_min_u16 v52, v52, v52 op_sel:[0,1] op_sel_hi:[1,0]\n"
    "s_add_u32 m0, s35, 0x16000\n"
    "s_nop 0\n"
    "buffer_load_dwordx4 v9, s[12:15], s57 offen lds\n"
    "v_pk_min_u16 v53, v53, v53 op_sel:[0,1] op_sel_hi:[1,0]\n"
    "v_pk_sub_i16 v48, v48, v52\n"
    "v_pk_sub_i16 v49, v49, v52\n"
    "v_pk_sub_i16 v50, v50, v53\n"
    "v_pk_sub_i16 v51, v51, v53\n"
    "v_pk_ashrrev_i16 v48, 15, v48 op_sel_hi:[0,1]\n"
    "s_add_u32 s31, s57, 0x8c00\n"
    "buffer_load_dwordx4 v9, s[12:15], s31 offen offset:1024 lds\n"
    "v_pk_ashrrev_i16 v49, 15, v49 op_sel_hi:[0,1]\n"
    "v_pk_ashrrev_i16 v50, 15, v50 op_sel_hi:[0,1]\n"
    "v_pk_ashrrev_i16 v51, 15, v51 op_sel_hi:[0,1]\n"
    "v_bitop3_b32 v16, v16, v48, v16 bitop3:0x30\n"
    "v_bitop3_b32 v17, v17, v49, v17 bitop3:0x30\n"
    "v_bitop3_b32 v18, v18, v50, v18 bitop3:0x30\n"
    "s_add_u32 s31, s57, 0x11800\n"
    "buffer_load_dwordx4 v9, s[12:15], s31 offen offset:2048 lds\n"
    "v_bitop3_b32 v19, v19, v51, v19 bitop3:0x30\n"
    "ds_write_b128 v3, v[16:19] offset:0\n"
    "s_add_u32 s31, s57, 0x1a400\n"
    "buffer_load_dwordx4 v9, s[12:15], s31 offen offset:3072 lds\n"
    "s_add_u32 m0, s35, 0x17000\n"
    "s_add_u32 s31, s57, 0x24000\n"
    "buffer_load_dwordx4 v9, s[12:15], s31 offen lds\n"
    "s_add_u32 s31, s57, 0x2cc00\n"
    "buffer_load_dwordx4 v9, s[12:15], s31 offen offset:1024 lds\n"
    "s_waitcnt vmcnt(8) lgkmcnt(0)\n"
    "s_barrier\n"
    "ds_read_b64_tr_b16 v[16:17], v2 offset:24576\n"
    "ds_read_b64_tr_b16 v[18:19], v2 offset:25088\n"
    "s_add_u32 m0, s35, 0x1c000\n"
    "s_nop 0\n"
    "buffer_load_dwordx4 v9, s[12:15], s58 offen lds\n"
    "s_waitcnt lgkmcnt(0)\n"
    "v_and_b32 v48, s39, v16\n"
    "v_and_b32 v49, s39, v17\n"
    "v_and_b32 v50, s39, v18\n"
    "v_and_b32_e64 v51, s39, v19\n"
    "v_pk_max_u16 v52, v48, v49\n"
    "s_add_u32 s31, s58, 0x8c00\n"
    "buffer_load_dwordx4 v9, s[12:15], s31 offen offset:1024 lds\n"
    "v_pk_max_u16 v53, v50, v51\n"
    "v_pk_min_u16 v54, v48, v49\n"
    "v_pk_min_u16 v55, v50, v51\n"
    "v_pk_max_u16 v52, v52, v54 op_sel:[0,1] op_sel_hi:[1,0]\n"
    "v_pk_max_u16 v53, v53, v55 op_sel:[0,1] op_sel_hi:[1,0]\n"
    "v_pk_min_u16 v52, v52, v52 op_sel:[0,1] op_sel_hi:[1,0]\n"
    "s_add_u32 s31, s58, 0x11800\n"
    "buffer_load_dwordx4 v9, s[12:15], s31 offen offset:2048 lds\n"
    "v_pk_min_u16 v53, v53, v53 op_sel:[0,1] op_sel_hi:[1,0]\n"
    "v_pk_sub_i16 v48, v48, v52\n"
    "v_pk_sub_i16 v49, v49, v52\n"
    "v_pk_sub_i16 v50, v50, v53\n"
    "v_pk_sub_i16 v51, v51, v53\n"
    "v_pk_ashrrev_i16 v48, 15, v48 op_sel_hi:[0,1]\n"
    "s_add_u32 s31, s58, 0x1a400\n"
    "buffer_load_dwordx4 v9, s[12:15], s31 offen offset:3072 lds\n"
    "v_pk_ashrrev_i16 v49, 15, v49 op_sel_hi:[0,1]\n"
    "v_pk_ashrrev_i16 v50, 15, v50 op_sel_hi:[0,1]\n"
    "v_pk_ashrrev_i16 v51, 15, v51 op_sel_hi:[0,1]\n"
    "v_bitop3_b32 v16, v16, v48, v16 bitop3:0x30\n"
    "v_bitop3_b32 v17, v17, v49, v17 bitop3:0x30\n"
    "v_bitop3_b32 v18, v18, v50, v18 bitop3:0x30\n"
    "s_add_u32 m0, s35, 0x1d000\n"
    "s_add_u32 s31, s58, 0x24000\n"
    "buffer_load_dwordx4 v9, s[12:15], s31 offen lds\n"
    "v_bitop3_b32 v19, v19, v51, v19 bitop3:0x30\n"
    "ds_write_b128 v3, v[16:19] offset:4096\n"
    "s_add_u32 s31, s58, 0x2cc00\n"
    "buffer_load_dwordx4 v9, s[12:15], s31 offen offset:1024 lds\n"
    "s_waitcnt vmcnt(6) lgkmcnt(0)\n"
    "s_barrier\n"
    "ds_read_b64_tr_b16 v[16:17], v2 offset:0\n"
    "ds_read_b64_tr_b16 v[18:19], v2 offset:512\n"
    "s_waitcnt lgkmcnt(0)\n"
    "v_and_b32 v48, s39, v16\n"
    "v_and_b32 v49, s39, v17\n"
    "v_and_b32 v50, s39, v18\n"
    "v_and_b32_e64 v51, s39, v19\n"
    "v_pk_max_u16 v52, v48, v49\n"
    "v_pk_max_u16 v53, v50, v51\n"
    "v_pk_min_u16 v54, v48, v49\n"
    "v_pk_min_u16 v55, v50, v51\n"
    "v_pk_max_u16 v52, v52, v54 op_sel:[0,1] op_sel_hi:[1,0]\n"
    "v_pk_max_u16 v53, v53, v55 op_sel:[0,1] op_sel_hi:[1,0]\n"
    "v_pk_min_u16 v52, v52, v52 op_sel:[0,1] op_sel_hi:[1,0]\n"
    "v_pk_min_u16 v53, v53, v53 op_sel:[0,1] op_sel_hi:[1,0]\n"
    "v_pk_sub_i16 v48, v48, v52\n"
    "v_pk_sub_i16 v49, v49, v52\n"
    "v_pk_sub_i16 v50, v50, v53\n"
    "v_pk_sub_i16 v51, v51, v53\n"
    "v_pk_ashrrev_i16 v48, 15, v48 op_sel_hi:[0,1]\n"
    "v_pk_ashrrev_i16 v49, 15, v49 op_sel_hi:[0,1]\n"
    "v_pk_ashrrev_i16 v50, 15, v50 op_sel_hi:[0,1]\n"
    "v_pk_ashrrev_i16 v51, 15, v51 op_sel_hi:[0,1]\n"
    "v_bitop3_b32 v16, v16, v48, v16 bitop3:0x30\n"
    "v_bitop3_b32 v17, v17, v49, v17 bitop3:0x30\n"
    "v_bitop3_b32 v18, v18, v50, v18 bitop3:0x30\n"
    "v_bitop3_b32 v19, v19, v51, v19 bitop3:0x30\n"
    "ds_write_b128 v3, v[16:19] offset:0\n"
    "s_waitcnt vmcnt(0) lgkmcnt(0)\n"
    "s_barrier\n"
    "s_branch .Lend%=\n"
    ".p2align 3\n"
    ".Lcons%=:\n"
    "s_setprio 1\n"
    "s_and_b32 s27, s27, 3\n"
    "s_getpc_b64 s[30:31]\n"
    ".Lpc%=:\n"
    "s_sub_u32 s30, s30, .Lpc%=-.Lstart%=\n"
    "s_subb_u32 s31, s31, 0\n"
    "s_bfe_u32 s28, %2, 0x20003\n"
    "s_lshl_b32 s28, s28, 2\n"
    "s_add_u32 s28, s28, s27\n"
    "s_lshl_b32 s28, s28, 11\n"
    "s_mov_b64 exec, 0xffff\n"
    "v_lshl_add_u32 v13, v1, 7, s28\n"
    "v_min_u32 v13, .Lend%=-.Lstart%=-4, v13\n"
    "global_load_dword v255, v13, s[30:31]\n"
    "s_mov_b64 exec, -1\n"
    "s_mul_i32 s28, s27, 1024\n"
    "s_add_u32 s28, s28, %3\n"
    "s_add_u32 s28, s28, 0x8000\n"
    "v_lshlrev_b32 v7, 4, v1\n"
    "v_add_u32_e64 v7, s28, v7\n"
    "s_mov_b32 s39, 0x7fff7fff\n"
    "v_lshrrev_b32_e64 v10, 5, v1\n"
    "v_bfe_u32 v11, v1, 1, 3\n"
    "v_xor_b32 v11, v11, v10\n"
    "v_lshlrev_b32 v11, 4, v11\n"
    "v_and_b32_e64 v12, 7, v1\n"
    "v_lshl_add_u32 v11, v12, 7, v11\n"
    "v_bfe_u32 v12, v1, 3, 2\n"
    "v_and_b32 v10, 32, v1\n"
    "s_movk_i32 s18, 0x480\n"
    "s_mov_b32 s19, 0x20000\n"
    "s_cmp_eq_u32 s37, 15\n"
    "s_cbranch_scc0 .Lcons_b%=\n"
    "s_lshl_b32 s28, s27, 6\n"
    "s_add_u32 s25, s25, s28\n"
    "s_mov_b32 s36, s24\n"
    "s_waitcnt lgkmcnt(0)\n"
    "s_mov_b32 s16, s8\n"
    "s_and_b32 s17, s9, 0xffff\n"
    "buffer_load_dwordx4 v[232:235], v10, s[16:19], s25 offen\n"
    "buffer_load_dwordx4 v[236:239], v10, s[16:19], s25 offen offset:16\n"
    "v_mul_u32_u24 v12, 0x1000, v12\n"
    "s_add_u32 s28, %3, 0x18000\n"
    "v_add3_u32 v2, v11, v12, s28\n"
    "v_xor_b32 v3, 32, v2\n"
    "v_xor_b32_e64 v4, 64, v2\n"
    "v_xor_b32 v5, 96, v2\n"
    "v_bfe_u32 v10, v1, 2, 2\n"
    "v_xor_b32 v11, s27, v10\n"
    "v_lshlrev_b32 v11, 6, v11\n"
    "v_lshl_add_u32 v11, v10, 8, v11\n"
    "v_lshrrev_b32_e64 v10, 5, v1\n"
    "v_lshl_add_u32 v11, v10, 11, v11\n"
    "v_and_b32_e64 v10, 1, v1\n"
    "v_lshl_add_u32 v11, v10, 5, v11\n"
    "v_and_b32 v10, 16, v1\n"
    "v_add_u32 v11, v11, v10\n"
    "v_bfe_u32 v10, v1, 1, 1\n"
    "v_lshl_add_u32 v11, v10, 3, v11\n"
    "v_add_u32 v6, %3, v11\n"
    "s_lshl_b32 s30, s27, 11\n"
    "s_add_u32 s30, s30, %3\n"
    "v_lshrrev_b32 v10, 4, v1\n"
    "v_and_b32_e64 v10, 2, v10\n"
    "v_bfe_u32 v11, v1, 1, 2\n"
    "v_xor_b32 v10, v10, v11\n"
    "v_lshlrev_b32 v10, 4, v10\n"
    "v_and_b32_e64 v11, 31, v1\n"
    "v_lshl_add_u32 v10, v11, 6, v10\n"
    "v_add_u32 v0, s30, v10\n"
    "s_barrier\n"
    "ds_read_b64_tr_b16 v[116:117], v6 offset:4096\n"
    "ds_read_b64_tr_b16 v[118:119], v6 offset:5120\n"
    "ds_read_b64_tr_b16 v[120:121], v6 offset:8192\n"
    "ds_read_b64_tr_b16 v[122:123], v6 offset:9216\n"
    "ds_read_b64_tr_b16 v[124:125], v6 offset:12288\n"
    "ds_read_b64_tr_b16 v[126:127], v6 offset:13312\n"
    "s_waitcnt lgkmcnt(4)\n"
    "v_and_b32 v8, s39, v116\n"
    "v_and_b32 v9, s39, v117\n"
    "v_and_b32 v10, s39, v118\n"
    "v_and_b32_e64 v11, s39, v119\n"
    "v_pk_max_u16 v12, v8, v9\n"
    "v_pk_max_u16 v13, v10, v11\n"
    "v_pk_min_u16 v14, v8, v9\n"
    "v_pk_min_u16 v15, v10, v11\n"
    "v_pk_max_u16 v12, v12, v14 op_sel:[0,1] op_sel_hi:[1,0]\n"
    "v_pk_max_u16 v13, v13, v15 op_sel:[0,1] op_sel_hi:[1,0]\n"
    "v_pk_min_u16 v12, v12, v12 op_sel:[0,1] op_sel_hi:[1,0]\n"
    "v_pk_min_u16 v13, v13, v13 op_sel:[0,1] op_sel_hi:[1,0]\n"
    "v_pk_sub_i16 v8, v8, v12\n"
    "v_pk_sub_i16 v9, v9, v12\n"
    "v_pk_sub_i16 v10, v10, v13\n"
    "v_pk_sub_i16 v11, v11, v13\n"
    "v_pk_ashrrev_i16 v8, 15, v8 op_sel_hi:[0,1]\n"
    "v_pk_ashrrev_i16 v9, 15, v9 op_sel_hi:[0,1]\n"
    "v_pk_ashrrev_i16 v10, 15, v10 op_sel_hi:[0,1]\n"
    "v_pk_ashrrev_i16 v11, 15, v11 op_sel_hi:[0,1]\n"
    "v_bitop3_b32 v116, v116, v8, v116 bitop3:0x30\n"
    "v_bitop3_b32 v117, v117, v9, v117 bitop3:0x30\n"
    "v_bitop3_b32 v118, v118, v10, v118 bitop3:0x30\n"
    "v_bitop3_b32 v119, v119, v11, v119 bitop3:0x30\n"
    "s_waitcnt lgkmcnt(2)\n"
    "v_and_b32 v8, s39, v120\n"
    "v_and_b32 v9, s39, v121\n"
    "v_and_b32 v10, s39, v122\n"
    "v_and_b32_e64 v11, s39, v123\n"
    "v_pk_max_u16 v12, v8, v9\n"
    "v_pk_max_u16 v13, v10, v11\n"
    "v_pk_min_u16 v14, v8, v9\n"
    "v_pk_min_u16 v15, v10, v11\n"
    "v_pk_max_u16 v12, v12, v14 op_sel:[0,1] op_sel_hi:[1,0]\n"
    "v_pk_max_u16 v13, v13, v15 op_sel:[0,1] op_sel_hi:[1,0]\n"
    "v_pk_min_u16 v12, v12, v12 op_sel:[0,1] op_sel_hi:[1,0]\n"
    "v_pk_min_u16 v13, v13, v13 op_sel:[0,1] op_sel_hi:[1,0]\n"
    "v_pk_sub_i16 v8, v8, v12\n"
    "v_pk_sub_i16 v9, v9, v12\n"
    "v_pk_sub_i16 v10, v10, v13\n"
    "v_pk_sub_i16 v11, v11, v13\n"
    "v_pk_ashrrev_i16 v8, 15, v8 op_sel_hi:[0,1]\n"
    "v_pk_ashrrev_i16 v9, 15, v9 op_sel_hi:[0,1]\n"
    "v_pk_ashrrev_i16 v10, 15, v10 op_sel_hi:[0,1]\n"
    "v_pk_ashrrev_i16 v11, 15, v11 op_sel_hi:[0,1]\n"
    "v_bitop3_b32 v120, v120, v8, v120 bitop3:0x30\n"
    "v_bitop3_b32 v121, v121, v9, v121 bitop3:0x30\n"
    "v_bitop3_b32 v122, v122, v10, v122 bitop3:0x30\n"
    "v_bitop3_b32 v123, v123, v11, v123 bitop3:0x30\n"
    "s_waitcnt lgkmcnt(0)\n"
    "v_and_b32 v8, s39, v124\n"
    "v_and_b32 v9, s39, v125\n"
    "v_and_b32 v10, s39, v126\n"
    "v_and_b32_e64 v11, s39, v127\n"
    "v_pk_max_u16 v12, v8, v9\n"
    "v_pk_max_u16 v13, v10, v11\n"
    "v_pk_min_u16 v14, v8, v9\n"
    "v_pk_min_u16 v15, v10, v11\n"
    "v_pk_max_u16 v12, v12, v14 op_sel:[0,1] op_sel_hi:[1,0]\n"
    "v_pk_max_u16 v13, v13, v15 op_sel:[0,1] op_sel_hi:[1,0]\n"
    "v_pk_min_u16 v12, v12, v12 op_sel:[0,1] op_sel_hi:[1,0]\n"
    "v_pk_min_u16 v13, v13, v13 op_sel:[0,1] op_sel_hi:[1,0]\n"
    "v_pk_sub_i16 v8, v8, v12\n"
    "v_pk_sub_i16 v9, v9, v12\n"
    "v_pk_sub_i16 v10, v10, v13\n"
    "v_pk_sub_i16 v11, v11, v13\n"
    "v_pk_ashrrev_i16 v8, 15, v8 op_sel_hi:[0,1]\n"
    "v_pk_ashrrev_i16 v9, 15, v9 op_sel_hi:[0,1]\n"
    "v_pk_ashrrev_i16 v10, 15, v10 op_sel_hi:[0,1]\n"
    "v_pk_ashrrev_i16 v11, 15, v11 op_sel_hi:[0,1]\n"
    "v_bitop3_b32 v124, v124, v8, v124 bitop3:0x30\n"
    "v_bitop3_b32 v125, v125, v9, v125 bitop3:0x30\n"
    "v_bitop3_b32 v126, v126, v10, v126 bitop3:0x30\n"
    "v_bitop3_b32 v127, v127, v11, v127 bitop3:0x30\n"
    "s_waitcnt vmcnt(0)\n"
    "v_and_b32 v97, 0xffff0000, v232\n"
    "v_lshlrev_b32 v96, 16, v232\n"
    "v_and_b32 v99, 0xffff0000, v233\n"
    "v_lshlrev_b32_e64 v98, 16, v233\n"
    "v_and_b32 v101, 0xffff0000, v234\n"
    "v_lshlrev_b32_e64 v100, 16, v234\n"
    "v_and_b32 v103, 0xffff0000, v235\n"
    "v_lshlrev_b32_e64 v102, 16, v235\n"
    "v_and_b32 v105, 0xffff0000, v236\n"
    "v_lshlrev_b32_e64 v104, 16, v236\n"
    "v_and_b32 v107, 0xffff0000, v237\n"
    "v_lshlrev_b32_e64 v106, 16, v237\n"
    "v_and_b32 v109, 0xffff0000, v238\n"
    "v_lshlrev_b32_e64 v108, 16, v238\n"
    "v_and_b32 v111, 0xffff0000, v239\n"
    "v_lshlrev_b32 v110, 16, v239\n"
    "s_mov_b32 s16, s10\n"
    "s_and_b32 s17, s11, 0xffff\n"
    "s_mov_b32 s18, 0x900000\n"
    "s_barrier\n"
    "ds_read_b128 v[112:115], v7 offset:32768\n"
    "ds_read_b128 v[16:19], v3 offset:0\n"
    "ds_read_b128 v[20:23], v3 offset:1024\n"
    "ds_read_b128 v[24:27], v3 offset:2048\n"
    "ds_read_b128 v[28:31], v3 offset:3072\n"
    "ds_read_b64_tr_b16 v[132:133], v6 offset:20480\n"
    "ds_read_b64_tr_b16 v[134:135], v6 offset:21504\n"
    "s_waitcnt lgkmcnt(4)\n"
    "v_mfma_f32_32x32x16_bf16 v[32:47], v[116:119], v[16:19], v[96:111]\n"
    "ds_read_b128 v[16:19], v4 offset:0\n"
    "v_mfma_f32_32x32x16_bf16 v[48:63], v[116:119], v[20:23], v[96:111]\n"
    "ds_read_b128 v[20:23], v4 offset:1024\n"
    "s_waitcnt lgkmcnt(2)\n"
    "v_and_b32 v8, s39, v132\n"
    "v_and_b32 v9, s39, v133\n"
    "v_and_b32 v10, s39, v134\n"
    "v_and_b32_e64 v11, s39, v135\n"
    "v_pk_max_u16 v12, v8, v9\n"
    "v_pk_max_u16 v13, v10, v11\n"
    "v_mfma_f32_32x32x16_bf16 v[64:79], v[116:119], v[24:27], v[96:111]\n"
    "ds_read_b128 v[24:27], v4 offset:2048\n"
    "v_pk_min_u16 v14, v8, v9\n"
    "v_pk_min_u16 v15, v10, v11\n"
    "v_pk_max_u16 v12, v12, v14 op_sel:[0,1] op_sel_hi:[1,0]\n"
    "v_pk_max_u16 v13, v13, v15 op_sel:[0,1] op_sel_hi:[1,0]\n"
    "v_pk_min_u16 v12, v12, v12 op_sel:[0,1] op_sel_hi:[1,0]\n"
    "v_pk_min_u16 v13, v13, v13 op_sel:[0,1] op_sel_hi:[1,0]\n"
    "v_pk_sub_i16 v8, v8, v12\n"
    "v_mfma_f32_32x32x16_bf16 v[80:95], v[116:119], v[28:31], v[96:111]\n"
    "ds_read_b128 v[28:31], v4 offset:3072\n"
    "v_pk_sub_i16 v9, v9, v12\n"
    "v_pk_sub_i16 v10, v10, v13\n"
    "v_pk_sub_i16 v11, v11, v13\n"
    "ds_read_b64_tr_b16 v[136:137], v6 offset:24576\n"
    "ds_read_b64_tr_b16 v[138:139], v6 offset:25600\n"
    "v_pk_ashrrev_i16 v8, 15, v8 op_sel_hi:[0,1]\n"
    "v_pk_ashrrev_i16 v9, 15, v9 op_sel_hi:[0,1]\n"
    "v_pk_ashrrev_i16 v10, 15, v10 op_sel_hi:[0,1]\n"
    "s_waitcnt lgkmcnt(4)\n"
    "v_mfma_f32_32x32x16_bf16 v[32:47], v[120:123], v[16:19], v[32:47]\n"
    "ds_read_b128 v[16:19], v5 offset:0\n"
    "v_pk_ashrrev_i16 v11, 15, v11 op_sel_hi:[0,1]\n"
    "v_bitop3_b32 v132, v132, v8, v132 bitop3:0x30\n"
    "v_bitop3_b32 v133, v133, v9, v133 bitop3:0x30\n"
    "v_bitop3_b32 v134, v134, v10, v134 bitop3:0x30\n"
    "v_bitop3_b32 v135, v135, v11, v135 bitop3:0x30\n"
    "s_waitcnt lgkmcnt(1)\n"
    "v_and_b32_e64 v8, s39, v136\n"
    "v_mfma_f32_32x32x16_bf16 v[48:63], v[120:123], v[20:23], v[48:63]\n"
    "ds_read_b128 v[20:23], v5 offset:1024\n"
    "v_and_b32 v9, s39, v137\n"
    "v_and_b32 v10, s39, v138\n"
    "v_and_b32_e64 v11, s39, v139\n"
    "v_pk_max_u16 v12, v8, v9\n"
    "v_pk_max_u16 v13, v10, v11\n"
    "v_pk_min_u16 v14, v8, v9\n"
    "v_pk_min_u16 v15, v10, v11\n"
    "v_mfma_f32_32x32x16_bf16 v[64:79], v[120:123], v[24:27], v[64:79]\n"
    "ds_read_b128 v[24:27], v5 offset:2048\n"
    "v_pk_max_u16 v12, v12, v14 op_sel:[0,1] op_sel_hi:[1,0]\n"
    "v_pk_max_u16 v13, v13, v15 op_sel:[0,1] op_sel_hi:[1,0]\n"
    "v_pk_min_u16 v12, v12, v12 op_sel:[0,1] op_sel_hi:[1,0]\n"
    "v_pk_min_u16 v13, v13, v13 op_sel:[0,1] op_sel_hi:[1,0]\n"
    "v_pk_sub_i16 v8, v8, v12\n"
    "v_pk_sub_i16 v9, v9, v12\n"
    "v_pk_sub_i16 v10, v10, v13\n"
    "v_mfma_f32_32x32x16_bf16 v[80:95], v[120:123], v[28:31], v[80:95]\n"
    "ds_read_b128 v[28:31], v5 offset:3072\n"
    "v_pk_sub_i16 v11, v11, v13\n"
    "ds_read_b64_tr_b16 v[140:141], v6 offset:28672\n"
    "ds_read_b64_tr_b16 v[142:143], v6 offset:29696\n"
    "v_pk_ashrrev_i16 v8, 15, v8 op_sel_hi:[0,1]\n"
    "v_pk_ashrrev_i16 v9, 15, v9 op_sel_hi:[0,1]\n"
    "v_pk_ashrrev_i16 v10, 15, v10 op_sel_hi:[0,1]\n"
    "v_pk_ashrrev_i16 v11, 15, v11 op_sel_hi:[0,1]\n"
    "v_bitop3_b32 v136, v136, v8, v136 bitop3:0x30\n"
    "s_waitcnt lgkmcnt(4)\n"
    "v_mfma_f32_32x32x16_bf16 v[32:47], v[124:127], v[16:19], v[32:47]\n"
    "ds_read_b128 v[16:19], v2 offset:0\n"
    "v_bitop3_b32 v137, v137, v9, v137 bitop3:0x30\n"
    "v_bitop3_b32 v138, v138, v10, v138 bitop3:0x30\n"
    "v_bitop3_b32 v139, v139, v11, v139 bitop3:0x30\n"
    "s_waitcnt lgkmcnt(1)\n"
    "v_and_b32 v8, s39, v140\n"
    "v_and_b32 v9, s39, v141\n"
    "v_and_b32_e64 v10, s39, v142\n"
    "v_mfma_f32_32x32x16_bf16 v[48:63], v[124:127], v[20:23], v[48:63]\n"
    "ds_read_b128 v[20:23], v2 offset:1024\n"
    "v_and_b32_e64 v11, s39, v143\n"
    "v_pk_max_u16 v12, v8, v9\n"
    "v_pk_max_u16 v13, v10, v11\n"
    "v_pk_min_u16 v14, v8, v9\n"
    "v_pk_min_u16 v15, v10, v11\n"
    "v_pk_max_u16 v12, v12, v14 op_sel:[0,1] op_sel_hi:[1,0]\n"
    "v_pk_max_u16 v13, v13, v15 op_sel:[0,1] op_sel_hi:[1,0]\n"
    "v_mfma_f32_32x32x16_bf16 v[64:79], v[124:127], v[24:27], v[64:79]\n"
    "ds_read_b128 v[24:27], v2 offset:2048\n"
    "v_pk_min_u16 v12, v12, v12 op_sel:[0,1] op_sel_hi:[1,0]\n"
    "v_pk_min_u16 v13, v13, v13 op_sel:[0,1] op_sel_hi:[1,0]\n"
    "v_pk_sub_i16 v8, v8, v12\n"
    "v_pk_sub_i16 v9, v9, v12\n"
    "v_pk_sub_i16 v10, v10, v13\n"
    "v_pk_sub_i16 v11, v11, v13\n"
    "v_pk_ashrrev_i16 v8, 15, v8 op_sel_hi:[0,1]\n"
    "v_mfma_f32_32x32x16_bf16 v[80:95], v[124:127], v[28:31], v[80:95]\n"
    "s_barrier\n"
    "ds_read_b128 v[128:131], v7 offset:36864\n"
    "ds_read_b128 v[28:31], v2 offset:3072\n"
    "v_pk_ashrrev_i16 v9, 15, v9 op_sel_hi:[0,1]\n"
    "v_pk_ashrrev_i16 v10, 15, v10 op_sel_hi:[0,1]\n"
    "v_pk_ashrrev_i16 v11, 15, v11 op_sel_hi:[0,1]\n"
    "v_bitop3_b32 v140, v140, v8, v140 bitop3:0x30\n"
    "v_bitop3_b32 v141, v141, v9, v141 bitop3:0x30\n"
    "v_bitop3_b32 v142, v142, v10, v142 bitop3:0x30\n"
    "v_bitop3_b32 v143, v143, v11, v143 bitop3:0x30\n"
    "ds_read_b64_tr_b16 v[148:149], v6 offset:36864\n"
    "ds_read_b64_tr_b16 v[150:151], v6 offset:37888\n"
    "s_waitcnt lgkmcnt(5)\n"
    "v_mfma_f32_32x32x16_bf16 v[32:47], v[112:115], v[16:19], v[32:47]\n"
    "ds_read_b128 v[16:19], v3 offset:16384\n"
    "v_mfma_f32_32x32x16_bf16 v[48:63], v[112:115], v[20:23], v[48:63]\n"
    "ds_read_b128 v[20:23], v3 offset:17408\n"
    "s_waitcnt lgkmcnt(4)\n"
    "v_mfma_f32_32x32x16_bf16 v[64:79], v[112:115], v[24:27], v[64:79]\n"
    "ds_read_b128 v[24:27], v3 offset:18432\n"
    "s_waitcnt lgkmcnt(3)\n"
    "v_and_b32 v8, s39, v148\n"
    "v_and_b32 v9, s39, v149\n"
    "v_and_b32 v10, s39, v150\n"
    "v_and_b32 v11, s39, v151\n"
    "v_pk_max_u16 v12, v8, v9\n"
    "v_mfma_f32_32x32x16_bf16 v[80:95], v[112:115], v[28:31], v[80:95]\n"
    "ds_read_b128 v[28:31], v3 offset:19456\n"
    "v_pk_max_u16 v13, v10, v11\n"
    "v_pk_min_u16 v14, v8, v9\n"
    "v_pk_min_u16 v15, v10, v11\n"
    "v_pk_max_u16 v12, v12, v14 op_sel:[0,1] op_sel_hi:[1,0]\n"
    "v_pk_max_u16 v13, v13, v15 op_sel:[0,1] op_sel_hi:[1,0]\n"
    "s_waitcnt lgkmcnt(2)\n"
    "v_mfma_f32_32x32x16_bf16 v[32:47], v[132:135], v[16:19], v[32:47]\n"
    "ds_read_b128 v[16:19], v4 offset:16384\n"
    "v_pk_min_u16 v12, v12, v12 op_sel:[0,1] op_sel_hi:[1,0]\n"
    "v_pk_min_u16 v13, v13, v13 op_sel:[0,1] op_sel_hi:[1,0]\n"
    "v_pk_sub_i16 v8, v8, v12\n"
    "v_pk_sub_i16 v9, v9, v12\n"
    "v_pk_sub_i16 v10, v10, v13\n"
    "v_pk_sub_i16 v11, v11, v13\n"
    "v_mfma_f32_32x32x16_bf16 v[48:63], v[132:135], v[20:23], v[48:63]\n"
    "ds_read_b128 v[20:23], v4 offset:17408\n"
    "ds_read_b64_tr_b16 v[152:153], v6 offset:40960\n"
    "ds_read_b64_tr_b16 v[154:155], v6 offset:41984\n"
    "v_pk_ashrrev_i16 v8, 15, v8 op_sel_hi:[0,1]\n"
    "v_pk_ashrrev_i16 v9, 15, v9 op_sel_hi:[0,1]\n"
    "v_pk_ashrrev_i16 v10, 15, v10 op_sel_hi:[0,1]\n"
    "v_pk_ashrrev_i16 v11, 15, v11 op_sel_hi:[0,1]\n"
    "s_waitcnt lgkmcnt(4)\n"
    "v_mfma_f32_32x32x16_bf16 v[64:79], v[132:135], v[24:27], v[64:79]\n"
    "ds_read_b128 v[24:27], v4 offset:18432\n"
    "v_bitop3_b32 v148, v148, v8, v148 bitop3:0x30\n"
    "v_bitop3_b32 v149, v149, v9, v149 bitop3:0x30\n"
    "v_bitop3_b32 v150, v150, v10, v150 bitop3:0x30\n"
    "v_bitop3_b32 v151, v151, v11, v151 bitop3:0x30\n"
    "s_waitcnt lgkmcnt(1)\n"
    "v_and_b32 v8, s39, v152\n"
    "v_mfma_f32_32x32x16_bf16 v[80:95], v[132:135], v[28:31], v[80:95]\n"
    "ds_read_b128 v[28:31], v4 offset:19456\n"
    "v_and_b32 v9, s39, v153\n"
    "v_and_b32 v10, s39, v154\n"
    "v_and_b32_e64 v11, s39, v155\n"
    "v_pk_max_u16 v12, v8, v9\n"
    "v_pk_max_u16 v13, v10, v11\n"
    "v_mfma_f32_32x32x16_bf16 v[32:47], v[136:139], v[16:19], v[32:47]\n"
    "ds_read_b128 v[16:19], v5 offset:16384\n"
    "v_pk_min_u16 v14, v8, v9\n"
    "v_pk_min_u16 v15, v10, v11\n"
    "v_pk_max_u16 v12, v12, v14 op_sel:[0,1] op_sel_hi:[1,0]\n"
    "v_pk_max_u16 v13, v13, v15 op_sel:[0,1] op_sel_hi:[1,0]\n"
    "v_pk_min_u16 v12, v12, v12 op_sel:[0,1] op_sel_hi:[1,0]\n"
    "v_pk_min_u16 v13, v13, v13 op_sel:[0,1] op_sel_hi:[1,0]\n"
    "v_mfma_f32_32x32x16_bf16 v[48:63], v[136:139], v[20:23], v[48:63]\n"
    "ds_read_b128 v[20:23], v5 offset:17408\n"
    "v_pk_sub_i16 v8, v8, v12\n"
    "v_pk_sub_i16 v9, v9, v12\n"
    "v_pk_sub_i16 v10, v10, v13\n"
    "v_pk_sub_i16 v11, v11, v13\n"
    "ds_read_b64_tr_b16 v[156:157], v6 offset:45056\n"
    "ds_read_b64_tr_b16 v[158:159], v6 offset:46080\n"
    "s_waitcnt lgkmcnt(4)\n"
    "v_mfma_f32_32x32x16_bf16 v[64:79], v[136:139], v[24:27], v[64:79]\n"
    "ds_read_b128 v[24:27], v5 offset:18432\n"
    "v_pk_ashrrev_i16 v8, 15, v8 op_sel_hi:[0,1]\n"
    "v_pk_ashrrev_i16 v9, 15, v9 op_sel_hi:[0,1]\n"
    "v_pk_ashrrev_i16 v10, 15, v10 op_sel_hi:[0,1]\n"
    "v_pk_ashrrev_i16 v11, 15, v11 op_sel_hi:[0,1]\n"
    "v_bitop3_b32 v152, v152, v8, v152 bitop3:0x30\n"
    "v_bitop3_b32 v153, v153, v9, v153 bitop3:0x30\n"
    "v_mfma_f32_32x32x16_bf16 v[80:95], v[136:139], v[28:31], v[80:95]\n"
    "ds_read_b128 v[28:31], v5 offset:19456\n"
    "v_bitop3_b32 v154, v154, v10, v154 bitop3:0x30\n"
    "v_bitop3_b32 v155, v155, v11, v155 bitop3:0x30\n"
    "s_waitcnt lgkmcnt(2)\n"
    "v_and_b32 v8, s39, v156\n"
    "v_and_b32 v9, s39, v157\n"
    "v_mfma_f32_32x32x16_bf16 v[32:47], v[140:143], v[16:19], v[32:47]\n"
    "ds_read_b128 v[16:19], v2 offset:16384\n"
    "v_and_b32 v10, s39, v158\n"
    "v_and_b32 v11, s39, v159\n"
    "v_pk_max_u16 v12, v8, v9\n"
    "v_pk_max_u16 v13, v10, v11\n"
    "v_pk_min_u16 v14, v8, v9\n"
    "v_pk_min_u16 v15, v10, v11\n"
    "v_mfma_f32_32x32x16_bf16 v[48:63], v[140:143], v[20:23], v[48:63]\n"
    "ds_read_b128 v[20:23], v2 offset:17408\n"
    "v_pk_max_u16 v12, v12, v14 op_sel:[0,1] op_sel_hi:[1,0]\n"
    "v_pk_max_u16 v13, v13, v15 op_sel:[0,1] op_sel_hi:[1,0]\n"
    "v_pk_min_u16 v12, v12, v12 op_sel:[0,1] op_sel_hi:[1,0]\n"
    "v_pk_min_u16 v13, v13, v13 op_sel:[0,1] op_sel_hi:[1,0]\n"
    "v_pk_sub_i16 v8, v8, v12\n"
    "s_waitcnt lgkmcnt(2)\n"
    "v_mfma_f32_32x32x16_bf16 v[64:79], v[140:143], v[24:27], v[64:79]\n"
    "ds_read_b128 v[24:27], v2 offset:18432\n"
    "v_pk_sub_i16 v9, v9, v12\n"
    "v_pk_sub_i16 v10, v10, v13\n"
    "v_pk_sub_i16 v11, v11, v13\n"
    "v_pk_ashrrev_i16 v8, 15, v8 op_sel_hi:[0,1]\n"
    "v_pk_ashrrev_i16 v9, 15, v9 op_sel_hi:[0,1]\n"
    "v_pk_ashrrev_i16 v10, 15, v10 op_sel_hi:[0,1]\n"
    "v_mfma_f32_32x32x16_bf16 v[80:95], v[140:143], v[28:31], v[80:95]\n"
    "s_barrier\n"
    "ds_read_b128 v[144:147], v7 offset:32768\n"
    "ds_read_b128 v[28:31], v2 offset:19456\n"
    "v_pk_ashrrev_i16 v11, 15, v11 op_sel_hi:[0,1]\n"
    "v_bitop3_b32 v156, v156, v8, v156 bitop3:0x30\n"
    "v_bitop3_b32 v157, v157, v9, v157 bitop3:0x30\n"
    "v_bitop3_b32 v158, v158, v10, v158 bitop3:0x30\n"
    "v_bitop3_b32 v159, v159, v11, v159 bitop3:0x30\n"
    "ds_read_b64_tr_b16 v[164:165], v6 offset:53248\n"
    "ds_read_b64_tr_b16 v[166:167], v6 offset:54272\n"
    "s_waitcnt lgkmcnt(5)\n"
    "v_mfma_f32_32x32x16_bf16 v[32:47], v[128:131], v[16:19], v[32:47]\n"
    "ds_read_b128 v[16:19], v3 offset:32768\n"
    "v_mfma_f32_32x32x16_bf16 v[48:63], v[128:131], v[20:23], v[48:63]\n"
    "ds_read_b128 v[20:23], v3 offset:33792\n"
    "s_waitcnt lgkmcnt(4)\n"
    "v_mfma_f32_32x32x16_bf16 v[64:79], v[128:131], v[24:27], v[64:79]\n"
    "ds_read_b128 v[24:27], v3 offset:34816\n"
    "s_waitcnt lgkmcnt(3)\n"
    "v_and_b32 v8, s39, v164\n"
    "v_and_b32 v9, s39, v165\n"
    "v_and_b32 v10, s39, v166\n"
    "v_and_b32_e64 v11, s39, v167\n"
    "v_pk_max_u16 v12, v8, v9\n"
    "v_mfma_f32_32x32x16_bf16 v[80:95], v[128:131], v[28:31], v[80:95]\n"
    "ds_read_b128 v[28:31], v3 offset:35840\n"
    "v_pk_max_u16 v13, v10, v11\n"
    "v_pk_min_u16 v14, v8, v9\n"
    "v_pk_min_u16 v15, v10, v11\n"
    "v_pk_max_u16 v12, v12, v14 op_sel:[0,1] op_sel_hi:[1,0]\n"
    "v_pk_max_u16 v13, v13, v15 op_sel:[0,1] op_sel_hi:[1,0]\n"
    "s_waitcnt lgkmcnt(2)\n"
    "v_mfma_f32_32x32x16_bf16 v[32:47], v[148:151], v[16:19], v[32:47]\n"
    "ds_read_b128 v[16:19], v4 offset:32768\n"
    "v_pk_min_u16 v12, v12, v12 op_sel:[0,1] op_sel_hi:[1,0]\n"
    "v_pk_min_u16 v13, v13, v13 op_sel:[0,1] op_sel_hi:[1,0]\n"
    "v_pk_sub_i16 v8, v8, v12\n"
    "v_pk_sub_i16 v9, v9, v12\n"
    "v_pk_sub_i16 v10, v10, v13\n"
    "v_pk_sub_i16 v11, v11, v13\n"
    "v_mfma_f32_32x32x16_bf16 v[48:63], v[148:151], v[20:23], v[48:63]\n"
    "ds_read_b128 v[20:23], v4 offset:33792\n"
    "ds_read_b64_tr_b16 v[168:169], v6 offset:57344\n"
    "ds_read_b64_tr_b16 v[170:171], v6 offset:58368\n"
    "v_pk_ashrrev_i16 v8, 15, v8 op_sel_hi:[0,1]\n"
    "v_pk_ashrrev_i16 v9, 15, v9 op_sel_hi:[0,1]\n"
    "v_pk_ashrrev_i16 v10, 15, v10 op_sel_hi:[0,1]\n"
    "v_pk_ashrrev_i16 v11, 15, v11 op_sel_hi:[0,1]\n"
    "s_waitcnt lgkmcnt(4)\n"
    "v_mfma_f32_32x32x16_bf16 v[64:79], v[148:151], v[24:27], v[64:79]\n"
    "ds_read_b128 v[24:27], v4 offset:34816\n"
    "v_bitop3_b32 v164, v164, v8, v164 bitop3:0x30\n"
    "v_bitop3_b32 v165, v165, v9, v165 bitop3:0x30\n"
    "v_bitop3_b32 v166, v166, v10, v166 bitop3:0x30\n"
    "v_bitop3_b32 v167, v167, v11, v167 bitop3:0x30\n"
    "s_waitcnt lgkmcnt(1)\n"
    "v_and_b32 v8, s39, v168\n"
    "v_mfma_f32_32x32x16_bf16 v[80:95], v[148:151], v[28:31], v[80:95]\n"
    "ds_read_b128 v[28:31], v4 offset:35840\n"
    "v_and_b32 v9, s39, v169\n"
    "v_and_b32 v10, s39, v170\n"
    "v_and_b32_e64 v11, s39, v171\n"
    "v_pk_max_u16 v12, v8, v9\n"
    "v_pk_max_u16 v13, v10, v11\n"
    "v_mfma_f32_32x32x16_bf16 v[32:47], v[152:155], v[16:19], v[32:47]\n"
    "ds_read_b128 v[16:19], v5 offset:32768\n"
    "v_pk_min_u16 v14, v8, v9\n"
    "v_pk_min_u16 v15, v10, v11\n"
    "v_pk_max_u16 v12, v12, v14 op_sel:[0,1] op_sel_hi:[1,0]\n"
    "v_pk_max_u16 v13, v13, v15 op_sel:[0,1] op_sel_hi:[1,0]\n"
    "v_pk_min_u16 v12, v12, v12 op_sel:[0,1] op_sel_hi:[1,0]\n"
    "v_pk_min_u16 v13, v13, v13 op_sel:[0,1] op_sel_hi:[1,0]\n"
    "v_mfma_f32_32x32x16_bf16 v[48:63], v[152:155], v[20:23], v[48:63]\n"
    "ds_read_b128 v[20:23], v5 offset:33792\n"
    "v_pk_sub_i16 v8, v8, v12\n"
    "v_pk_sub_i16 v9, v9, v12\n"
    "v_pk_sub_i16 v10, v10, v13\n"
    "v_pk_sub_i16 v11, v11, v13\n"
    "ds_read_b64_tr_b16 v[172:173], v6 offset:61440\n"
    "ds_read_b64_tr_b16 v[174:175], v6 offset:62464\n"
    "s_waitcnt lgkmcnt(4)\n"
    "v_mfma_f32_32x32x16_bf16 v[64:79], v[152:155], v[24:27], v[64:79]\n"
    "ds_read_b128 v[24:27], v5 offset:34816\n"
    "v_pk_ashrrev_i16 v8, 15, v8 op_sel_hi:[0,1]\n"
    "v_pk_ashrrev_i16 v9, 15, v9 op_sel_hi:[0,1]\n"
    "v_pk_ashrrev_i16 v10, 15, v10 op_sel_hi:[0,1]\n"
    "v_pk_ashrrev_i16 v11, 15, v11 op_sel_hi:[0,1]\n"
    "v_bitop3_b32 v168, v168, v8, v168 bitop3:0x30\n"
    "v_bitop3_b32 v169, v169, v9, v169 bitop3:0x30\n"
    "v_mfma_f32_32x32x16_bf16 v[80:95], v[152:155], v[28:31], v[80:95]\n"
    "ds_read_b128 v[28:31], v5 offset:35840\n"
    "v_bitop3_b32 v170, v170, v10, v170 bitop3:0x30\n"
    "v_bitop3_b32 v171, v171, v11, v171 bitop3:0x30\n"
    "s_waitcnt lgkmcnt(2)\n"
    "v_and_b32 v8, s39, v172\n"
    "v_and_b32 v9, s39, v173\n"
    "v_mfma_f32_32x32x16_bf16 v[32:47], v[156:159], v[16:19], v[32:47]\n"
    "ds_read_b128 v[16:19], v2 offset:32768\n"
    "v_and_b32 v10, s39, v174\n"
    "v_and_b32 v11, s39, v175\n"
    "v_pk_max_u16 v12, v8, v9\n"
    "v_pk_max_u16 v13, v10, v11\n"
    "v_pk_min_u16 v14, v8, v9\n"
    "v_pk_min_u16 v15, v10, v11\n"
    "v_mfma_f32_32x32x16_bf16 v[48:63], v[156:159], v[20:23], v[48:63]\n"
    "ds_read_b128 v[20:23], v2 offset:33792\n"
    "v_pk_max_u16 v12, v12, v14 op_sel:[0,1] op_sel_hi:[1,0]\n"
    "v_pk_max_u16 v13, v13, v15 op_sel:[0,1] op_sel_hi:[1,0]\n"
    "v_pk_min_u16 v12, v12, v12 op_sel:[0,1] op_sel_hi:[1,0]\n"
    "v_pk_min_u16 v13, v13, v13 op_sel:[0,1] op_sel_hi:[1,0]\n"
    "v_pk_sub_i16 v8, v8, v12\n"
    "s_waitcnt lgkmcnt(2)\n"
    "v_mfma_f32_32x32x16_bf16 v[64:79], v[156:159], v[24:27], v[64:79]\n"
    "ds_read_b128 v[24:27], v2 offset:34816\n"
    "v_pk_sub_i16 v9, v9, v12\n"
    "v_pk_sub_i16 v10, v10, v13\n"
    "v_pk_sub_i16 v11, v11, v13\n"
    "v_pk_ashrrev_i16 v8, 15, v8 op_sel_hi:[0,1]\n"
    "v_pk_ashrrev_i16 v9, 15, v9 op_sel_hi:[0,1]\n"
    "v_pk_ashrrev_i16 v10, 15, v10 op_sel_hi:[0,1]\n"
    "v_mfma_f32_32x32x16_bf16 v[80:95], v[156:159], v[28:31], v[80:95]\n"
    "s_barrier\n"
    "ds_read_b128 v[160:163], v7 offset:36864\n"
    "ds_read_b128 v[28:31], v2 offset:35840\n"
    "v_pk_ashrrev_i16 v11, 15, v11 op_sel_hi:[0,1]\n"
    "v_bitop3_b32 v172, v172, v8, v172 bitop3:0x30\n"
    "v_bitop3_b32 v173, v173, v9, v173 bitop3:0x30\n"
    "v_bitop3_b32 v174, v174, v10, v174 bitop3:0x30\n"
    "v_bitop3_b32 v175, v175, v11, v175 bitop3:0x30\n"
    "ds_read_b64_tr_b16 v[180:181], v6 offset:4096\n"
    "ds_read_b64_tr_b16 v[182:183], v6 offset:5120\n"
    "s_waitcnt lgkmcnt(5)\n"
    "v_mfma_f32_32x32x16_bf16 v[32:47], v[144:147], v[16:19], v[32:47]\n"
    "ds_read_b128 v[16:19], v3 offset:49152\n"
    "v_mfma_f32_32x32x16_bf16 v[48:63], v[144:147], v[20:23], v[48:63]\n"
    "ds_read_b128 v[20:23], v3 offset:50176\n"
    "s_waitcnt lgkmcnt(4)\n"
    "v_mfma_f32_32x32x16_bf16 v[64:79], v[144:147], v[24:27], v[64:79]\n"
    "ds_read_b128 v[24:27], v3 offset:51200\n"
    "s_waitcnt lgkmcnt(3)\n"
    "v_and_b32 v8, s39, v180\n"
    "v_and_b32 v9, s39, v181\n"
    "v_and_b32 v10, s39, v182\n"
    "v_and_b32_e64 v11, s39, v183\n"
    "v_pk_max_u16 v12, v8, v9\n"
    "v_mfma_f32_32x32x16_bf16 v[80:95], v[144:147], v[28:31], v[80:95]\n"
    "ds_read_b128 v[28:31], v3 offset:52224\n"
    "v_pk_max_u16 v13, v10, v11\n"
    "v_pk_min_u16 v14, v8, v9\n"
    "v_pk_min_u16 v15, v10, v11\n"
    "v_pk_max_u16 v12, v12, v14 op_sel:[0,1] op_sel_hi:[1,0]\n"
    "v_pk_max_u16 v13, v13, v15 op_sel:[0,1] op_sel_hi:[1,0]\n"
    "s_waitcnt lgkmcnt(2)\n"
    "v_mfma_f32_32x32x16_bf16 v[32:47], v[164:167], v[16:19], v[32:47]\n"
    "ds_read_b128 v[16:19], v4 offset:49152\n"
    "v_pk_min_u16 v12, v12, v12 op_sel:[0,1] op_sel_hi:[1,0]\n"
    "v_pk_min_u16 v13, v13, v13 op_sel:[0,1] op_sel_hi:[1,0]\n"
    "v_pk_sub_i16 v8, v8, v12\n"
    "v_pk_sub_i16 v9, v9, v12\n"
    "v_pk_sub_i16 v10, v10, v13\n"
    "v_pk_sub_i16 v11, v11, v13\n"
    "v_mfma_f32_32x32x16_bf16 v[48:63], v[164:167], v[20:23], v[48:63]\n"
    "ds_read_b128 v[20:23], v4 offset:50176\n"
    "ds_read_b64_tr_b16 v[184:185], v6 offset:8192\n"
    "ds_read_b64_tr_b16 v[186:187], v6 offset:9216\n"
    "v_pk_ashrrev_i16 v8, 15, v8 op_sel_hi:[0,1]\n"
    "v_pk_ashrrev_i16 v9, 15, v9 op_sel_hi:[0,1]\n"
    "v_pk_ashrrev_i16 v10, 15, v10 op_sel_hi:[0,1]\n"
    "v_pk_ashrrev_i16 v11, 15, v11 op_sel_hi:[0,1]\n"
    "s_waitcnt lgkmcnt(4)\n"
    "v_mfma_f32_32x32x16_bf16 v[64:79], v[164:167], v[24:27], v[64:79]\n"
    "ds_read_b128 v[24:27], v4 offset:51200\n"
    "v_bitop3_b32 v180, v180, v8, v180 bitop3:0x30\n"
    "v_bitop3_b32 v181, v181, v9, v181 bitop3:0x30\n"
    "v_bitop3_b32 v182, v182, v10, v182 bitop3:0x30\n"
    "v_bitop3_b32 v183, v183, v11, v183 bitop3:0x30\n"
    "s_waitcnt lgkmcnt(1)\n"
    "v_and_b32 v8, s39, v184\n"
    "v_mfma_f32_32x32x16_bf16 v[80:95], v[164:167], v[28:31], v[80:95]\n"
    "ds_read_b128 v[28:31], v4 offset:52224\n"
    "v_and_b32 v9, s39, v185\n"
    "v_and_b32 v10, s39, v186\n"
    "v_and_b32_e64 v11, s39, v187\n"
    "v_pk_max_u16 v12, v8, v9\n"
    "v_pk_max_u16 v13, v10, v11\n"
    "v_mfma_f32_32x32x16_bf16 v[32:47], v[168:171], v[16:19], v[32:47]\n"
    "ds_read_b128 v[16:19], v5 offset:49152\n"
    "v_pk_min_u16 v14, v8, v9\n"
    "v_pk_min_u16 v15, v10, v11\n"
    "v_pk_max_u16 v12, v12, v14 op_sel:[0,1] op_sel_hi:[1,0]\n"
    "v_pk_max_u16 v13, v13, v15 op_sel:[0,1] op_sel_hi:[1,0]\n"
    "v_pk_min_u16 v12, v12, v12 op_sel:[0,1] op_sel_hi:[1,0]\n"
    "v_pk_min_u16 v13, v13, v13 op_sel:[0,1] op_sel_hi:[1,0]\n"
    "v_mfma_f32_32x32x16_bf16 v[48:63], v[168:171], v[20:23], v[48:63]\n"
    "ds_read_b128 v[20:23], v5 offset:50176\n"
    "v_pk_sub_i16 v8, v8, v12\n"
    "v_pk_sub_i16 v9, v9, v12\n"
    "v_pk_sub_i16 v10, v10, v13\n"
    "v_pk_sub_i16 v11, v11, v13\n"
    "ds_read_b64_tr_b16 v[188:189], v6 offset:12288\n"
    "ds_read_b64_tr_b16 v[190:191], v6 offset:13312\n"
    "s_waitcnt lgkmcnt(4)\n"
    "v_mfma_f32_32x32x16_bf16 v[64:79], v[168:171], v[24:27], v[64:79]\n"
    "ds_read_b128 v[24:27], v5 offset:51200\n"
    "v_pk_ashrrev_i16 v8, 15, v8 op_sel_hi:[0,1]\n"
    "v_pk_ashrrev_i16 v9, 15, v9 op_sel_hi:[0,1]\n"
    "v_pk_ashrrev_i16 v10, 15, v10 op_sel_hi:[0,1]\n"
    "v_pk_ashrrev_i16 v11, 15, v11 op_sel_hi:[0,1]\n"
    "v_bitop3_b32 v184, v184, v8, v184 bitop3:0x30\n"
    "v_bitop3_b32 v185, v185, v9, v185 bitop3:0x30\n"
    "v_mfma_f32_32x32x16_bf16 v[80:95], v[168:171], v[28:31], v[80:95]\n"
    "ds_read_b128 v[28:31], v5 offset:52224\n"
    "v_bitop3_b32 v186, v186, v10, v186 bitop3:0x30\n"
    "v_bitop3_b32 v187, v187, v11, v187 bitop3:0x30\n"
    "s_waitcnt lgkmcnt(2)\n"
    "v_and_b32 v8, s39, v188\n"
    "v_and_b32 v9, s39, v189\n"
    "v_mfma_f32_32x32x16_bf16 v[32:47], v[172:175], v[16:19], v[32:47]\n"
    "ds_read_b128 v[16:19], v2 offset:49152\n"
    "v_and_b32 v10, s39, v190\n"
    "v_and_b32 v11, s39, v191\n"
    "v_pk_max_u16 v12, v8, v9\n"
    "v_pk_max_u16 v13, v10, v11\n"
    "v_pk_min_u16 v14, v8, v9\n"
    "v_pk_min_u16 v15, v10, v11\n"
    "v_mfma_f32_32x32x16_bf16 v[48:63], v[172:175], v[20:23], v[48:63]\n"
    "ds_read_b128 v[20:23], v2 offset:50176\n"
    "v_pk_max_u16 v12, v12, v14 op_sel:[0,1] op_sel_hi:[1,0]\n"
    "v_pk_max_u16 v13, v13, v15 op_sel:[0,1] op_sel_hi:[1,0]\n"
    "v_pk_min_u16 v12, v12, v12 op_sel:[0,1] op_sel_hi:[1,0]\n"
    "v_pk_min_u16 v13, v13, v13 op_sel:[0,1] op_sel_hi:[1,0]\n"
    "v_pk_sub_i16 v8, v8, v12\n"
    "s_waitcnt lgkmcnt(2)\n"
    "v_mfma_f32_32x32x16_bf16 v[64:79], v[172:175], v[24:27], v[64:79]\n"
    "ds_read_b128 v[24:27], v2 offset:51200\n"
    "v_pk_sub_i16 v9, v9, v12\n"
    "v_pk_sub_i16 v10, v10, v13\n"
    "v_pk_sub_i16 v11, v11, v13\n"
    "v_pk_ashrrev_i16 v8, 15, v8 op_sel_hi:[0,1]\n"
    "v_pk_ashrrev_i16 v9, 15, v9 op_sel_hi:[0,1]\n"
    "v_pk_ashrrev_i16 v10, 15, v10 op_sel_hi:[0,1]\n"
    "v_mfma_f32_32x32x16_bf16 v[80:95], v[172:175], v[28:31], v[80:95]\n"
    "s_barrier\n"
    "ds_read_b128 v[176:179], v7 offset:32768\n"
    "ds_read_b128 v[28:31], v2 offset:52224\n"
    "v_pk_ashrrev_i16 v11, 15, v11 op_sel_hi:[0,1]\n"
    "v_bitop3_b32 v188, v188, v8, v188 bitop3:0x30\n"
    "v_bitop3_b32 v189, v189, v9, v189 bitop3:0x30\n"
    "v_bitop3_b32 v190, v190, v10, v190 bitop3:0x30\n"
    "v_bitop3_b32 v191, v191, v11, v191 bitop3:0x30\n"
    "ds_read_b64_tr_b16 v[196:197], v6 offset:20480\n"
    "ds_read_b64_tr_b16 v[198:199], v6 offset:21504\n"
    "s_waitcnt lgkmcnt(5)\n"
    "v_mfma_f32_32x32x16_bf16 v[32:47], v[160:163], v[16:19], v[32:47]\n"
    "ds_read_b128 v[16:19], v3 offset:0\n"
    "v_mfma_f32_32x32x16_bf16 v[48:63], v[160:163], v[20:23], v[48:63]\n"
    "ds_read_b128 v[20:23], v3 offset:1024\n"
    "s_waitcnt lgkmcnt(4)\n"
    "v_mfma_f32_32x32x16_bf16 v[64:79], v[160:163], v[24:27], v[64:79]\n"
    "ds_read_b128 v[24:27], v3 offset:2048\n"
    "s_waitcnt lgkmcnt(3)\n"
    "v_and_b32 v8, s39, v196\n"
    "v_and_b32 v9, s39, v197\n"
    "v_and_b32 v10, s39, v198\n"
    "v_and_b32_e64 v11, s39, v199\n"
    "v_pk_max_u16 v12, v8, v9\n"
    "v_mfma_f32_32x32x16_bf16 v[80:95], v[160:163], v[28:31], v[80:95]\n"
    "ds_read_b128 v[28:31], v3 offset:3072\n"
    "v_pk_max_u16 v13, v10, v11\n"
    "v_pk_min_u16 v14, v8, v9\n"
    "v_pk_min_u16 v15, v10, v11\n"
    "v_pk_max_u16 v12, v12, v14 op_sel:[0,1] op_sel_hi:[1,0]\n"
    "v_pk_max_u16 v13, v13, v15 op_sel:[0,1] op_sel_hi:[1,0]\n"
    "s_waitcnt lgkmcnt(2)\n"
    "v_mfma_f32_32x32x16_bf16 v[32:47], v[180:183], v[16:19], v[32:47]\n"
    "ds_read_b128 v[16:19], v4 offset:0\n"
    "v_pk_min_u16 v12, v12, v12 op_sel:[0,1] op_sel_hi:[1,0]\n"
    "v_pk_min_u16 v13, v13, v13 op_sel:[0,1] op_sel_hi:[1,0]\n"
    "v_pk_sub_i16 v8, v8, v12\n"
    "v_pk_sub_i16 v9, v9, v12\n"
    "v_pk_sub_i16 v10, v10, v13\n"
    "v_pk_sub_i16 v11, v11, v13\n"
    "v_mfma_f32_32x32x16_bf16 v[48:63], v[180:183], v[20:23], v[48:63]\n"
    "ds_read_b128 v[20:23], v4 offset:1024\n"
    "ds_read_b64_tr_b16 v[200:201], v6 offset:24576\n"
    "ds_read_b64_tr_b16 v[202:203], v6 offset:25600\n"
    "v_pk_ashrrev_i16 v8, 15, v8 op_sel_hi:[0,1]\n"
    "v_pk_ashrrev_i16 v9, 15, v9 op_sel_hi:[0,1]\n"
    "v_pk_ashrrev_i16 v10, 15, v10 op_sel_hi:[0,1]\n"
    "v_pk_ashrrev_i16 v11, 15, v11 op_sel_hi:[0,1]\n"
    "s_waitcnt lgkmcnt(4)\n"
    "v_mfma_f32_32x32x16_bf16 v[64:79], v[180:183], v[24:27], v[64:79]\n"
    "ds_read_b128 v[24:27], v4 offset:2048\n"
    "v_bitop3_b32 v196, v196, v8, v196 bitop3:0x30\n"
    "v_bitop3_b32 v197, v197, v9, v197 bitop3:0x30\n"
    "v_bitop3_b32 v198, v198, v10, v198 bitop3:0x30\n"
    "v_bitop3_b32 v199, v199, v11, v199 bitop3:0x30\n"
    "s_waitcnt lgkmcnt(1)\n"
    "v_and_b32 v8, s39, v200\n"
    "v_mfma_f32_32x32x16_bf16 v[80:95], v[180:183], v[28:31], v[80:95]\n"
    "ds_read_b128 v[28:31], v4 offset:3072\n"
    "v_and_b32 v9, s39, v201\n"
    "v_and_b32 v10, s39, v202\n"
    "v_and_b32_e64 v11, s39, v203\n"
    "v_pk_max_u16 v12, v8, v9\n"
    "v_pk_max_u16 v13, v10, v11\n"
    "v_mfma_f32_32x32x16_bf16 v[32:47], v[184:187], v[16:19], v[32:47]\n"
    "ds_read_b128 v[16:19], v5 offset:0\n"
    "v_pk_min_u16 v14, v8, v9\n"
    "v_pk_min_u16 v15, v10, v11\n"
    "v_pk_max_u16 v12, v12, v14 op_sel:[0,1] op_sel_hi:[1,0]\n"
    "v_pk_max_u16 v13, v13, v15 op_sel:[0,1] op_sel_hi:[1,0]\n"
    "v_pk_min_u16 v12, v12, v12 op_sel:[0,1] op_sel_hi:[1,0]\n"
    "v_pk_min_u16 v13, v13, v13 op_sel:[0,1] op_sel_hi:[1,0]\n"
    "v_mfma_f32_32x32x16_bf16 v[48:63], v[184:187], v[20:23], v[48:63]\n"
    "ds_read_b128 v[20:23], v5 offset:1024\n"
    "v_pk_sub_i16 v8, v8, v12\n"
    "v_pk_sub_i16 v9, v9, v12\n"
    "v_pk_sub_i16 v10, v10, v13\n"
    "v_pk_sub_i16 v11, v11, v13\n"
    "ds_read_b64_tr_b16 v[204:205], v6 offset:28672\n"
    "ds_read_b64_tr_b16 v[206:207], v6 offset:29696\n"
    "s_waitcnt lgkmcnt(4)\n"
    "v_mfma_f32_32x32x16_bf16 v[64:79], v[184:187], v[24:27], v[64:79]\n"
    "ds_read_b128 v[24:27], v5 offset:2048\n"
    "v_pk_ashrrev_i16 v8, 15, v8 op_sel_hi:[0,1]\n"
    "v_pk_ashrrev_i16 v9, 15, v9 op_sel_hi:[0,1]\n"
    "v_pk_ashrrev_i16 v10, 15, v10 op_sel_hi:[0,1]\n"
    "v_pk_ashrrev_i16 v11, 15, v11 op_sel_hi:[0,1]\n"
    "v_bitop3_b32 v200, v200, v8, v200 bitop3:0x30\n"
    "v_bitop3_b32 v201, v201, v9, v201 bitop3:0x30\n"
    "v_mfma_f32_32x32x16_bf16 v[80:95], v[184:187], v[28:31], v[80:95]\n"
    "ds_read_b128 v[28:31], v5 offset:3072\n"
    "v_bitop3_b32 v202, v202, v10, v202 bitop3:0x30\n"
    "v_bitop3_b32 v203, v203, v11, v203 bitop3:0x30\n"
    "s_waitcnt lgkmcnt(2)\n"
    "v_and_b32 v8, s39, v204\n"
    "v_and_b32 v9, s39, v205\n"
    "v_mfma_f32_32x32x16_bf16 v[32:47], v[188:191], v[16:19], v[32:47]\n"
    "ds_read_b128 v[16:19], v2 offset:0\n"
    "v_and_b32 v10, s39, v206\n"
    "v_and_b32 v11, s39, v207\n"
    "v_pk_max_u16 v12, v8, v9\n"
    "v_pk_max_u16 v13, v10, v11\n"
    "v_pk_min_u16 v14, v8, v9\n"
    "v_pk_min_u16 v15, v10, v11\n"
    "v_mfma_f32_32x32x16_bf16 v[48:63], v[188:191], v[20:23], v[48:63]\n"
    "ds_read_b128 v[20:23], v2 offset:1024\n"
    "v_pk_max_u16 v12, v12, v14 op_sel:[0,1] op_sel_hi:[1,0]\n"
    "v_pk_max_u16 v13, v13, v15 op_sel:[0,1] op_sel_hi:[1,0]\n"
    "v_pk_min_u16 v12, v12, v12 op_sel:[0,1] op_sel_hi:[1,0]\n"
    "v_pk_min_u16 v13, v13, v13 op_sel:[0,1] op_sel_hi:[1,0]\n"
    "v_pk_sub_i16 v8, v8, v12\n"
    "s_waitcnt lgkmcnt(2)\n"
    "v_mfma_f32_32x32x16_bf16 v[64:79], v[188:191], v[24:27], v[64:79]\n"
    "ds_read_b128 v[24:27], v2 offset:2048\n"
    "v_pk_sub_i16 v9, v9, v12\n"
    "v_pk_sub_i16 v10, v10, v13\n"
    "v_pk_sub_i16 v11, v11, v13\n"
    "v_pk_ashrrev_i16 v8, 15, v8 op_sel_hi:[0,1]\n"
    "v_pk_ashrrev_i16 v9, 15, v9 op_sel_hi:[0,1]\n"
    "v_pk_ashrrev_i16 v10, 15, v10 op_sel_hi:[0,1]\n"
    "v_mfma_f32_32x32x16_bf16 v[80:95], v[188:191], v[28:31], v[80:95]\n"
    "s_waitcnt vmcnt(0)\n"
    "s_barrier\n"
    "ds_read_b128 v[192:195], v7 offset:36864\n"
    "ds_read_b128 v[28:31], v2 offset:3072\n"
    "v_pk_ashrrev_i16 v11, 15, v11 op_sel_hi:[0,1]\n"
    "v_bitop3_b32 v204, v204, v8, v204 bitop3:0x30\n"
    "v_bitop3_b32 v205, v205, v9, v205 bitop3:0x30\n"
    "v_bitop3_b32 v206, v206, v10, v206 bitop3:0x30\n"
    "v_bitop3_b32 v207, v207, v11, v207 bitop3:0x30\n"
    "ds_read_b64_tr_b16 v[212:213], v6 offset:36864\n"
    "ds_read_b64_tr_b16 v[214:215], v6 offset:37888\n"
    "s_waitcnt lgkmcnt(5)\n"
    "v_mfma_f32_32x32x16_bf16 v[32:47], v[176:179], v[16:19], v[32:47]\n"
    "ds_read_b128 v[16:19], v3 offset:16384\n"
    "v_mfma_f32_32x32x16_bf16 v[48:63], v[176:179], v[20:23], v[48:63]\n"
    "ds_read_b128 v[20:23], v3 offset:17408\n"
    "s_waitcnt lgkmcnt(4)\n"
    "v_mfma_f32_32x32x16_bf16 v[64:79], v[176:179], v[24:27], v[64:79]\n"
    "ds_read_b128 v[24:27], v3 offset:18432\n"
    "s_waitcnt lgkmcnt(3)\n"
    "v_and_b32 v8, s39, v212\n"
    "v_and_b32 v9, s39, v213\n"
    "v_and_b32 v10, s39, v214\n"
    "v_and_b32 v11, s39, v215\n"
    "v_pk_max_u16 v12, v8, v9\n"
    "v_mfma_f32_32x32x16_bf16 v[80:95], v[176:179], v[28:31], v[80:95]\n"
    "ds_read_b128 v[28:31], v3 offset:19456\n"
    "v_pk_max_u16 v13, v10, v11\n"
    "v_pk_min_u16 v14, v8, v9\n"
    "v_pk_min_u16 v15, v10, v11\n"
    "v_pk_max_u16 v12, v12, v14 op_sel:[0,1] op_sel_hi:[1,0]\n"
    "v_pk_max_u16 v13, v13, v15 op_sel:[0,1] op_sel_hi:[1,0]\n"
    "s_waitcnt lgkmcnt(2)\n"
    "v_mfma_f32_32x32x16_bf16 v[32:47], v[196:199], v[16:19], v[32:47]\n"
    "ds_read_b128 v[16:19], v4 offset:16384\n"
    "v_pk_min_u16 v12, v12, v12 op_sel:[0,1] op_sel_hi:[1,0]\n"
    "v_pk_min_u16 v13, v13, v13 op_sel:[0,1] op_sel_hi:[1,0]\n"
    "v_pk_sub_i16 v8, v8, v12\n"
    "v_pk_sub_i16 v9, v9, v12\n"
    "v_pk_sub_i16 v10, v10, v13\n"
    "v_pk_sub_i16 v11, v11, v13\n"
    "v_mfma_f32_32x32x16_bf16 v[48:63], v[196:199], v[20:23], v[48:63]\n"
    "ds_read_b128 v[20:23], v4 offset:17408\n"
    "ds_read_b64_tr_b16 v[216:217], v6 offset:40960\n"
    "ds_read_b64_tr_b16 v[218:219], v6 offset:41984\n"
    "v_pk_ashrrev_i16 v8, 15, v8 op_sel_hi:[0,1]\n"
    "v_pk_ashrrev_i16 v9, 15, v9 op_sel_hi:[0,1]\n"
    "v_pk_ashrrev_i16 v10, 15, v10 op_sel_hi:[0,1]\n"
    "v_pk_ashrrev_i16 v11, 15, v11 op_sel_hi:[0,1]\n"
    "s_waitcnt lgkmcnt(4)\n"
    "v_mfma_f32_32x32x16_bf16 v[64:79], v[196:199], v[24:27], v[64:79]\n"
    "ds_read_b128 v[24:27], v4 offset:18432\n"
    "v_bitop3_b32 v212, v212, v8, v212 bitop3:0x30\n"
    "v_bitop3_b32 v213, v213, v9, v213 bitop3:0x30\n"
    "v_bitop3_b32 v214, v214, v10, v214 bitop3:0x30\n"
    "v_bitop3_b32 v215, v215, v11, v215 bitop3:0x30\n"
    "s_waitcnt lgkmcnt(1)\n"
    "v_and_b32 v8, s39, v216\n"
    "v_mfma_f32_32x32x16_bf16 v[80:95], v[196:199], v[28:31], v[80:95]\n"
    "ds_read_b128 v[28:31], v4 offset:19456\n"
    "v_and_b32 v9, s39, v217\n"
    "v_and_b32 v10, s39, v218\n"
    "v_and_b32_e64 v11, s39, v219\n"
    "v_pk_max_u16 v12, v8, v9\n"
    "v_pk_max_u16 v13, v10, v11\n"
    "v_mfma_f32_32x32x16_bf16 v[32:47], v[200:203], v[16:19], v[32:47]\n"
    "ds_read_b128 v[16:19], v5 offset:16384\n"
    "v_pk_min_u16 v14, v8, v9\n"
    "v_pk_min_u16 v15, v10, v11\n"
    "v_pk_max_u16 v12, v12, v14 op_sel:[0,1] op_sel_hi:[1,0]\n"
    "v_pk_max_u16 v13, v13, v15 op_sel:[0,1] op_sel_hi:[1,0]\n"
    "v_pk_min_u16 v12, v12, v12 op_sel:[0,1] op_sel_hi:[1,0]\n"
    "v_pk_min_u16 v13, v13, v13 op_sel:[0,1] op_sel_hi:[1,0]\n"
    "v_mfma_f32_32x32x16_bf16 v[48:63], v[200:203], v[20:23], v[48:63]\n"
    "ds_read_b128 v[20:23], v5 offset:17408\n"
    "v_pk_sub_i16 v8, v8, v12\n"
    "v_pk_sub_i16 v9, v9, v12\n"
    "v_pk_sub_i16 v10, v10, v13\n"
    "v_pk_sub_i16 v11, v11, v13\n"
    "ds_read_b64_tr_b16 v[220:221], v6 offset:45056\n"
    "ds_read_b64_tr_b16 v[222:223], v6 offset:46080\n"
    "s_waitcnt lgkmcnt(4)\n"
    "v_mfma_f32_32x32x16_bf16 v[64:79], v[200:203], v[24:27], v[64:79]\n"
    "ds_read_b128 v[24:27], v5 offset:18432\n"
    "v_pk_ashrrev_i16 v8, 15, v8 op_sel_hi:[0,1]\n"
    "v_pk_ashrrev_i16 v9, 15, v9 op_sel_hi:[0,1]\n"
    "v_pk_ashrrev_i16 v10, 15, v10 op_sel_hi:[0,1]\n"
    "v_pk_ashrrev_i16 v11, 15, v11 op_sel_hi:[0,1]\n"
    "v_bitop3_b32 v216, v216, v8, v216 bitop3:0x30\n"
    "v_bitop3_b32 v217, v217, v9, v217 bitop3:0x30\n"
    "v_mfma_f32_32x32x16_bf16 v[80:95], v[200:203], v[28:31], v[80:95]\n"
    "ds_read_b128 v[28:31], v5 offset:19456\n"
    "v_bitop3_b32 v218, v218, v10, v218 bitop3:0x30\n"
    "v_bitop3_b32 v219, v219, v11, v219 bitop3:0x30\n"
    "s_waitcnt lgkmcnt(2)\n"
    "v_and_b32 v8, s39, v220\n"
    "v_and_b32 v9, s39, v221\n"
    "v_mfma_f32_32x32x16_bf16 v[32:47], v[204:207], v[16:19], v[32:47]\n"
    "ds_read_b128 v[16:19], v2 offset:16384\n"
    "v_and_b32 v10, s39, v222\n"
    "v_and_b32 v11, s39, v223\n"
    "v_pk_max_u16 v12, v8, v9\n"
    "v_pk_max_u16 v13, v10, v11\n"
    "v_pk_min_u16 v14, v8, v9\n"
    "v_pk_min_u16 v15, v10, v11\n"
    "v_mfma_f32_32x32x16_bf16 v[48:63], v[204:207], v[20:23], v[48:63]\n"
    "ds_read_b128 v[20:23], v2 offset:17408\n"
    "v_pk_max_u16 v12, v12, v14 op_sel:[0,1] op_sel_hi:[1,0]\n"
    "v_pk_max_u16 v13, v13, v15 op_sel:[0,1] op_sel_hi:[1,0]\n"
    "v_pk_min_u16 v12, v12, v12 op_sel:[0,1] op_sel_hi:[1,0]\n"
    "v_pk_min_u16 v13, v13, v13 op_sel:[0,1] op_sel_hi:[1,0]\n"
    "v_pk_sub_i16 v8, v8, v12\n"
    "s_waitcnt lgkmcnt(2)\n"
    "v_mfma_f32_32x32x16_bf16 v[64:79], v[204:207], v[24:27], v[64:79]\n"
    "ds_read_b128 v[24:27], v2 offset:18432\n"
    "v_pk_sub_i16 v9, v9, v12\n"
    "v_pk_sub_i16 v10, v10, v13\n"
    "v_pk_sub_i16 v11, v11, v13\n"
    "v_pk_ashrrev_i16 v8, 15, v8 op_sel_hi:[0,1]\n"
    "v_pk_ashrrev_i16 v9, 15, v9 op_sel_hi:[0,1]\n"
    "v_pk_ashrrev_i16 v10, 15, v10 op_sel_hi:[0,1]\n"
    "v_mfma_f32_32x32x16_bf16 v[80:95], v[204:207], v[28:31], v[80:95]\n"
    "s_barrier\n"
    "ds_read_b128 v[208:211], v7 offset:32768\n"
    "ds_read_b128 v[28:31], v2 offset:19456\n"
    "v_pk_ashrrev_i16 v11, 15, v11 op_sel_hi:[0,1]\n"
    "v_bitop3_b32 v220, v220, v8, v220 bitop3:0x30\n"
    "v_bitop3_b32 v221, v221, v9, v221 bitop3:0x30\n"
    "v_bitop3_b32 v222, v222, v10, v222 bitop3:0x30\n"
    "v_bitop3_b32 v223, v223, v11, v223 bitop3:0x30\n"
    "ds_read_b64_tr_b16 v[228:229], v6 offset:53248\n"
    "ds_read_b64_tr_b16 v[230:231], v6 offset:54272\n"
    "s_waitcnt lgkmcnt(5)\n"
    "v_mfma_f32_32x32x16_bf16 v[32:47], v[192:195], v[16:19], v[32:47]\n"
    "ds_read_b128 v[16:19], v3 offset:32768\n"
    "v_mfma_f32_32x32x16_bf16 v[48:63], v[192:195], v[20:23], v[48:63]\n"
    "ds_read_b128 v[20:23], v3 offset:33792\n"
    "s_waitcnt lgkmcnt(4)\n"
    "v_mfma_f32_32x32x16_bf16 v[64:79], v[192:195], v[24:27], v[64:79]\n"
    "ds_read_b128 v[24:27], v3 offset:34816\n"
    "s_waitcnt lgkmcnt(3)\n"
    "v_and_b32 v8, s39, v228\n"
    "v_and_b32 v9, s39, v229\n"
    "v_and_b32 v10, s39, v230\n"
    "v_and_b32_e64 v11, s39, v231\n"
    "v_pk_max_u16 v12, v8, v9\n"
    "v_mfma_f32_32x32x16_bf16 v[80:95], v[192:195], v[28:31], v[80:95]\n"
    "ds_read_b128 v[28:31], v3 offset:35840\n"
    "v_pk_max_u16 v13, v10, v11\n"
    "v_pk_min_u16 v14, v8, v9\n"
    "v_pk_min_u16 v15, v10, v11\n"
    "v_pk_max_u16 v12, v12, v14 op_sel:[0,1] op_sel_hi:[1,0]\n"
    "v_pk_max_u16 v13, v13, v15 op_sel:[0,1] op_sel_hi:[1,0]\n"
    "s_waitcnt lgkmcnt(2)\n"
    "v_mfma_f32_32x32x16_bf16 v[32:47], v[212:215], v[16:19], v[32:47]\n"
    "ds_read_b128 v[16:19], v4 offset:32768\n"
    "v_pk_min_u16 v12, v12, v12 op_sel:[0,1] op_sel_hi:[1,0]\n"
    "v_pk_min_u16 v13, v13, v13 op_sel:[0,1] op_sel_hi:[1,0]\n"
    "v_pk_sub_i16 v8, v8, v12\n"
    "v_pk_sub_i16 v9, v9, v12\n"
    "v_pk_sub_i16 v10, v10, v13\n"
    "v_pk_sub_i16 v11, v11, v13\n"
    "v_mfma_f32_32x32x16_bf16 v[48:63], v[212:215], v[20:23], v[48:63]\n"
    "ds_read_b128 v[20:23], v4 offset:33792\n"
    "ds_read_b64_tr_b16 v[232:233], v6 offset:57344\n"
    "ds_read_b64_tr_b16 v[234:235], v6 offset:58368\n"
    "v_pk_ashrrev_i16 v8, 15, v8 op_sel_hi:[0,1]\n"
    "v_pk_ashrrev_i16 v9, 15, v9 op_sel_hi:[0,1]\n"
    "v_pk_ashrrev_i16 v10, 15, v10 op_sel_hi:[0,1]\n"
    "v_pk_ashrrev_i16 v11, 15, v11 op_sel_hi:[0,1]\n"
    "s_waitcnt lgkmcnt(4)\n"
    "v_mfma_f32_32x32x16_bf16 v[64:79], v[212:215], v[24:27], v[64:79]\n"
    "ds_read_b128 v[24:27], v4 offset:34816\n"
    "v_bitop3_b32 v228, v228, v8, v228 bitop3:0x30\n"
    "v_bitop3_b32 v229, v229, v9, v229 bitop3:0x30\n"
    "v_bitop3_b32 v230, v230, v10, v230 bitop3:0x30\n"
    "v_bitop3_b32 v231, v231, v11, v231 bitop3:0x30\n"
    "s_waitcnt lgkmcnt(1)\n"
    "v_and_b32 v8, s39, v232\n"
    "v_mfma_f32_32x32x16_bf16 v[80:95], v[212:215], v[28:31], v[80:95]\n"
    "ds_read_b128 v[28:31], v4 offset:35840\n"
    "v_and_b32 v9, s39, v233\n"
    "v_and_b32 v10, s39, v234\n"
    "v_and_b32_e64 v11, s39, v235\n"
    "v_pk_max_u16 v12, v8, v9\n"
    "v_pk_max_u16 v13, v10, v11\n"
    "v_mfma_f32_32x32x16_bf16 v[32:47], v[216:219], v[16:19], v[32:47]\n"
    "ds_read_b128 v[16:19], v5 offset:32768\n"
    "v_pk_min_u16 v14, v8, v9\n"
    "v_pk_min_u16 v15, v10, v11\n"
    "v_pk_max_u16 v12, v12, v14 op_sel:[0,1] op_sel_hi:[1,0]\n"
    "v_pk_max_u16 v13, v13, v15 op_sel:[0,1] op_sel_hi:[1,0]\n"
    "v_pk_min_u16 v12, v12, v12 op_sel:[0,1] op_sel_hi:[1,0]\n"
    "v_pk_min_u16 v13, v13, v13 op_sel:[0,1] op_sel_hi:[1,0]\n"
    "v_mfma_f32_32x32x16_bf16 v[48:63], v[216:219], v[20:23], v[48:63]\n"
    "ds_read_b128 v[20:23], v5 offset:33792\n"
    "v_pk_sub_i16 v8, v8, v12\n"
    "v_pk_sub_i16 v9, v9, v12\n"
    "v_pk_sub_i16 v10, v10, v13\n"
    "v_pk_sub_i16 v11, v11, v13\n"
    "ds_read_b64_tr_b16 v[236:237], v6 offset:61440\n"
    "ds_read_b64_tr_b16 v[238:239], v6 offset:62464\n"
    "s_waitcnt lgkmcnt(4)\n"
    "v_mfma_f32_32x32x16_bf16 v[64:79], v[216:219], v[24:27], v[64:79]\n"
    "ds_read_b128 v[24:27], v5 offset:34816\n"
    "v_pk_ashrrev_i16 v8, 15, v8 op_sel_hi:[0,1]\n"
    "v_pk_ashrrev_i16 v9, 15, v9 op_sel_hi:[0,1]\n"
    "v_pk_ashrrev_i16 v10, 15, v10 op_sel_hi:[0,1]\n"
    "v_pk_ashrrev_i16 v11, 15, v11 op_sel_hi:[0,1]\n"
    "v_bitop3_b32 v232, v232, v8, v232 bitop3:0x30\n"
    "v_bitop3_b32 v233, v233, v9, v233 bitop3:0x30\n"
    "v_mfma_f32_32x32x16_bf16 v[80:95], v[216:219], v[28:31], v[80:95]\n"
    "ds_read_b128 v[28:31], v5 offset:35840\n"
    "v_bitop3_b32 v234, v234, v10, v234 bitop3:0x30\n"
    "v_bitop3_b32 v235, v235, v11, v235 bitop3:0x30\n"
    "s_waitcnt lgkmcnt(2)\n"
    "v_and_b32 v8, s39, v236\n"
    "v_and_b32 v9, s39, v237\n"
    "v_mfma_f32_32x32x16_bf16 v[32:47], v[220:223], v[16:19], v[32:47]\n"
    "ds_read_b128 v[16:19], v2 offset:32768\n"
    "v_and_b32 v10, s39, v238\n"
    "v_and_b32 v11, s39, v239\n"
    "v_pk_max_u16 v12, v8, v9\n"
    "v_pk_max_u16 v13, v10, v11\n"
    "v_pk_min_u16 v14, v8, v9\n"
    "v_pk_min_u16 v15, v10, v11\n"
    "v_mfma_f32_32x32x16_bf16 v[48:63], v[220:223], v[20:23], v[48:63]\n"
    "ds_read_b128 v[20:23], v2 offset:33792\n"
    "v_pk_max_u16 v12, v12, v14 op_sel:[0,1] op_sel_hi:[1,0]\n"
    "v_pk_max_u16 v13, v13, v15 op_sel:[0,1] op_sel_hi:[1,0]\n"
    "v_pk_min_u16 v12, v12, v12 op_sel:[0,1] op_sel_hi:[1,0]\n"
    "v_pk_min_u16 v13, v13, v13 op_sel:[0,1] op_sel_hi:[1,0]\n"
    "v_pk_sub_i16 v8, v8, v12\n"
    "s_waitcnt lgkmcnt(2)\n"
    "v_mfma_f32_32x32x16_bf16 v[64:79], v[220:223], v[24:27], v[64:79]\n"
    "ds_read_b128 v[24:27], v2 offset:34816\n"
    "v_pk_sub_i16 v9, v9, v12\n"
    "v_pk_sub_i16 v10, v10, v13\n"
    "v_pk_sub_i16 v11, v11, v13\n"
    "v_pk_ashrrev_i16 v8, 15, v8 op_sel_hi:[0,1]\n"
    "v_pk_ashrrev_i16 v9, 15, v9 op_sel_hi:[0,1]\n"
    "v_pk_ashrrev_i16 v10, 15, v10 op_sel_hi:[0,1]\n"
    "v_mfma_f32_32x32x16_bf16 v[80:95], v[220:223], v[28:31], v[80:95]\n"
    "s_barrier\n"
    "ds_read_b128 v[224:227], v7 offset:36864\n"
    "ds_read_b128 v[28:31], v2 offset:35840\n"
    "v_pk_ashrrev_i16 v11, 15, v11 op_sel_hi:[0,1]\n"
    "v_bitop3_b32 v236, v236, v8, v236 bitop3:0x30\n"
    "v_bitop3_b32 v237, v237, v9, v237 bitop3:0x30\n"
    "v_bitop3_b32 v238, v238, v10, v238 bitop3:0x30\n"
    "v_bitop3_b32 v239, v239, v11, v239 bitop3:0x30\n"
    "ds_read_b64_tr_b16 v[244:245], v6 offset:4096\n"
    "ds_read_b64_tr_b16 v[246:247], v6 offset:5120\n"
    "s_waitcnt lgkmcnt(5)\n"
    "v_mfma_f32_32x32x16_bf16 v[32:47], v[208:211], v[16:19], v[32:47]\n"
    "ds_read_b128 v[16:19], v3 offset:49152\n"
    "v_mfma_f32_32x32x16_bf16 v[48:63], v[208:211], v[20:23], v[48:63]\n"
    "ds_read_b128 v[20:23], v3 offset:50176\n"
    "s_waitcnt lgkmcnt(4)\n"
    "v_mfma_f32_32x32x16_bf16 v[64:79], v[208:211], v[24:27], v[64:79]\n"
    "ds_read_b128 v[24:27], v3 offset:51200\n"
    "s_waitcnt lgkmcnt(3)\n"
    "v_and_b32 v8, s39, v244\n"
    "v_and_b32 v9, s39, v245\n"
    "v_and_b32 v10, s39, v246\n"
    "v_and_b32_e64 v11, s39, v247\n"
    "v_pk_max_u16 v12, v8, v9\n"
    "v_mfma_f32_32x32x16_bf16 v[80:95], v[208:211], v[28:31], v[80:95]\n"
    "ds_read_b128 v[28:31], v3 offset:52224\n"
    "v_pk_max_u16 v13, v10, v11\n"
    "v_pk_min_u16 v14, v8, v9\n"
    "v_pk_min_u16 v15, v10, v11\n"
    "v_pk_max_u16 v12, v12, v14 op_sel:[0,1] op_sel_hi:[1,0]\n"
    "v_pk_max_u16 v13, v13, v15 op_sel:[0,1] op_sel_hi:[1,0]\n"
    "s_waitcnt lgkmcnt(2)\n"
    "v_mfma_f32_32x32x16_bf16 v[32:47], v[228:231], v[16:19], v[32:47]\n"
    "ds_read_b128 v[16:19], v4 offset:49152\n"
    "v_pk_min_u16 v12, v12, v12 op_sel:[0,1] op_sel_hi:[1,0]\n"
    "v_pk_min_u16 v13, v13, v13 op_sel:[0,1] op_sel_hi:[1,0]\n"
    "v_pk_sub_i16 v8, v8, v12\n"
    "v_pk_sub_i16 v9, v9, v12\n"
    "v_pk_sub_i16 v10, v10, v13\n"
    "v_pk_sub_i16 v11, v11, v13\n"
    "v_mfma_f32_32x32x16_bf16 v[48:63], v[228:231], v[20:23], v[48:63]\n"
    "ds_read_b128 v[20:23], v4 offset:50176\n"
    "ds_read_b64_tr_b16 v[248:249], v6 offset:8192\n"
    "ds_read_b64_tr_b16 v[250:251], v6 offset:9216\n"
    "v_pk_ashrrev_i16 v8, 15, v8 op_sel_hi:[0,1]\n"
    "v_pk_ashrrev_i16 v9, 15, v9 op_sel_hi:[0,1]\n"
    "v_pk_ashrrev_i16 v10, 15, v10 op_sel_hi:[0,1]\n"
    "v_pk_ashrrev_i16 v11, 15, v11 op_sel_hi:[0,1]\n"
    "s_waitcnt lgkmcnt(4)\n"
    "v_mfma_f32_32x32x16_bf16 v[64:79], v[228:231], v[24:27], v[64:79]\n"
    "ds_read_b128 v[24:27], v4 offset:51200\n"
    "v_bitop3_b32 v244, v244, v8, v244 bitop3:0x30\n"
    "v_bitop3_b32 v245, v245, v9, v245 bitop3:0x30\n"
    "v_bitop3_b32 v246, v246, v10, v246 bitop3:0x30\n"
    "v_bitop3_b32 v247, v247, v11, v247 bitop3:0x30\n"
    "s_waitcnt lgkmcnt(1)\n"
    "v_and_b32 v8, s39, v248\n"
    "v_mfma_f32_32x32x16_bf16 v[80:95], v[228:231], v[28:31], v[80:95]\n"
    "ds_read_b128 v[28:31], v4 offset:52224\n"
    "v_and_b32 v9, s39, v249\n"
    "v_and_b32 v10, s39, v250\n"
    "v_and_b32_e64 v11, s39, v251\n"
    "v_pk_max_u16 v12, v8, v9\n"
    "v_pk_max_u16 v13, v10, v11\n"
    "v_mfma_f32_32x32x16_bf16 v[32:47], v[232:235], v[16:19], v[32:47]\n"
    "ds_read_b128 v[16:19], v5 offset:49152\n"
    "v_pk_min_u16 v14, v8, v9\n"
    "v_pk_min_u16 v15, v10, v11\n"
    "v_pk_max_u16 v12, v12, v14 op_sel:[0,1] op_sel_hi:[1,0]\n"
    "v_pk_max_u16 v13, v13, v15 op_sel:[0,1] op_sel_hi:[1,0]\n"
    "v_pk_min_u16 v12, v12, v12 op_sel:[0,1] op_sel_hi:[1,0]\n"
    "v_pk_min_u16 v13, v13, v13 op_sel:[0,1] op_sel_hi:[1,0]\n"
    "v_mfma_f32_32x32x16_bf16 v[48:63], v[232:235], v[20:23], v[48:63]\n"
    "ds_read_b128 v[20:23], v5 offset:50176\n"
    "v_pk_sub_i16 v8, v8, v12\n"
    "v_pk_sub_i16 v9, v9, v12\n"
    "v_pk_sub_i16 v10, v10, v13\n"
    "v_pk_sub_i16 v11, v11, v13\n"
    "ds_read_b64_tr_b16 v[252:253], v6 offset:12288\n"
    "ds_read_b64_tr_b16 v[254:255], v6 offset:13312\n"
    "s_waitcnt lgkmcnt(4)\n"
    "v_mfma_f32_32x32x16_bf16 v[64:79], v[232:235], v[24:27], v[64:79]\n"
    "ds_read_b128 v[24:27], v5 offset:51200\n"
    "v_pk_ashrrev_i16 v8, 15, v8 op_sel_hi:[0,1]\n"
    "v_pk_ashrrev_i16 v9, 15, v9 op_sel_hi:[0,1]\n"
    "v_pk_ashrrev_i16 v10, 15, v10 op_sel_hi:[0,1]\n"
    "v_pk_ashrrev_i16 v11, 15, v11 op_sel_hi:[0,1]\n"
    "v_bitop3_b32 v248, v248, v8, v248 bitop3:0x30\n"
    "v_bitop3_b32 v249, v249, v9, v249 bitop3:0x30\n"
    "v_mfma_f32_32x32x16_bf16 v[80:95], v[232:235], v[28:31], v[80:95]\n"
    "ds_read_b128 v[28:31], v5 offset:52224\n"
    "v_bitop3_b32 v250, v250, v10, v250 bitop3:0x30\n"
    "v_bitop3_b32 v251, v251, v11, v251 bitop3:0x30\n"
    "s_waitcnt lgkmcnt(2)\n"
    "v_and_b32 v8, s39, v252\n"
    "v_and_b32 v9, s39, v253\n"
    "v_mfma_f32_32x32x16_bf16 v[32:47], v[236:239], v[16:19], v[32:47]\n"
    "ds_read_b128 v[16:19], v2 offset:49152\n"
    "v_and_b32 v10, s39, v254\n"
    "v_and_b32 v11, s39, v255\n"
    "v_pk_max_u16 v12, v8, v9\n"
    "v_pk_max_u16 v13, v10, v11\n"
    "v_pk_min_u16 v14, v8, v9\n"
    "v_pk_min_u16 v15, v10, v11\n"
    "v_mfma_f32_32x32x16_bf16 v[48:63], v[236:239], v[20:23], v[48:63]\n"
    "ds_read_b128 v[20:23], v2 offset:50176\n"
    "v_pk_max_u16 v12, v12, v14 op_sel:[0,1] op_sel_hi:[1,0]\n"
    "v_pk_max_u16 v13, v13, v15 op_sel:[0,1] op_sel_hi:[1,0]\n"
    "v_pk_min_u16 v12, v12, v12 op_sel:[0,1] op_sel_hi:[1,0]\n"
    "v_pk_min_u16 v13, v13, v13 op_sel:[0,1] op_sel_hi:[1,0]\n"
    "v_pk_sub_i16 v8, v8, v12\n"
    "s_waitcnt lgkmcnt(2)\n"
    "v_mfma_f32_32x32x16_bf16 v[64:79], v[236:239], v[24:27], v[64:79]\n"
    "ds_read_b128 v[24:27], v2 offset:51200\n"
    "v_pk_sub_i16 v9, v9, v12\n"
    "v_pk_sub_i16 v10, v10, v13\n"
    "v_pk_sub_i16 v11, v11, v13\n"
    "v_pk_ashrrev_i16 v8, 15, v8 op_sel_hi:[0,1]\n"
    "v_pk_ashrrev_i16 v9, 15, v9 op_sel_hi:[0,1]\n"
    "v_pk_ashrrev_i16 v10, 15, v10 op_sel_hi:[0,1]\n"
    "v_mfma_f32_32x32x16_bf16 v[80:95], v[236:239], v[28:31], v[80:95]\n"
    "s_barrier\n"
    "ds_read_b128 v[240:243], v7 offset:32768\n"
    "ds_read_b128 v[28:31], v2 offset:52224\n"
    "v_pk_ashrrev_i16 v11, 15, v11 op_sel_hi:[0,1]\n"
    "v_bitop3_b32 v252, v252, v8, v252 bitop3:0x30\n"
    "v_bitop3_b32 v253, v253, v9, v253 bitop3:0x30\n"
    "v_bitop3_b32 v254, v254, v10, v254 bitop3:0x30\n"
    "v_bitop3_b32 v255, v255, v11, v255 bitop3:0x30\n"
    "s_waitcnt lgkmcnt(3)\n"
    "v_mfma_f32_32x32x16_bf16 v[32:47], v[224:227], v[16:19], v[32:47]\n"
    "ds_read_b128 v[16:19], v3 offset:0\n"
    "v_subrev_u32 v12, 0x4000, v2\n"
    "v_subrev_u32 v13, 0x4000, v3\n"
    "v_subrev_u32 v14, 0x4000, v4\n"
    "v_subrev_u32 v15, 0x4000, v5\n"
    "v_mfma_f32_32x32x16_bf16 v[48:63], v[224:227], v[20:23], v[48:63]\n"
    "ds_read_b128 v[20:23], v3 offset:1024\n"
    "s_mov_b32 s28, s30\n"
    "s_waitcnt lgkmcnt(2)\n"
    "v_mfma_f32_32x32x16_bf16 v[64:79], v[224:227], v[24:27], v[64:79]\n"
    "ds_read_b128 v[24:27], v3 offset:2048\n"
    "v_lshrrev_b32 v10, 3, v1\n"
    "v_mfma_f32_32x32x16_bf16 v[80:95], v[224:227], v[28:31], v[80:95]\n"
    "ds_read_b128 v[28:31], v3 offset:3072\n"
    "v_xor_b32 v10, v10, v1\n"
    "s_waitcnt lgkmcnt(2)\n"
    "v_mfma_f32_32x32x16_bf16 v[32:47], v[244:247], v[16:19], v[32:47]\n"
    "ds_read_b128 v[16:19], v4 offset:0\n"
    "v_and_b32_e64 v10, 3, v10\n"
    "v_mfma_f32_32x32x16_bf16 v[48:63], v[244:247], v[20:23], v[48:63]\n"
    "ds_read_b128 v[20:23], v4 offset:1024\n"
    "v_lshlrev_b32 v10, 4, v10\n"
    "s_waitcnt lgkmcnt(2)\n"
    "v_mfma_f32_32x32x16_bf16 v[64:79], v[244:247], v[24:27], v[64:79]\n"
    "ds_read_b128 v[24:27], v4 offset:2048\n"
    "v_lshrrev_b32_e64 v11, 2, v1\n"
    "v_mfma_f32_32x32x16_bf16 v[80:95], v[244:247], v[28:31], v[80:95]\n"
    "ds_read_b128 v[28:31], v4 offset:3072\n"
    "v_lshl_add_u32 v10, v11, 6, v10\n"
    "s_waitcnt lgkmcnt(2)\n"
    "v_mfma_f32_32x32x16_bf16 v[32:47], v[248:251], v[16:19], v[32:47]\n"
    "ds_read_b128 v[16:19], v5 offset:0\n"
    "v_add_u32 v8, s28, v10\n"
    "v_mfma_f32_32x32x16_bf16 v[48:63], v[248:251], v[20:23], v[48:63]\n"
    "ds_read_b128 v[20:23], v5 offset:1024\n"
    "v_mul_u32_u24 v11, 0x480, v11\n"
    "s_waitcnt lgkmcnt(2)\n"
    "v_mfma_f32_32x32x16_bf16 v[64:79], v[248:251], v[24:27], v[64:79]\n"
    "ds_read_b128 v[24:27], v5 offset:2048\n"
    "v_mfma_f32_32x32x16_bf16 v[80:95], v[248:251], v[28:31], v[80:95]\n"
    "ds_read_b128 v[28:31], v5 offset:3072\n"
    "v_and_b32_e64 v10, 3, v1\n"
    "s_waitcnt lgkmcnt(2)\n"
    "v_mfma_f32_32x32x16_bf16 v[32:47], v[252:255], v[16:19], v[32:47]\n"
    "ds_read_b128 v[16:19], v2 offset:0\n"
    "v_lshl_add_u32 v11, v10, 4, v11\n"
    "v_mfma_f32_32x32x16_bf16 v[48:63], v[252:255], v[20:23], v[48:63]\n"
    "ds_read_b128 v[20:23], v2 offset:1024\n"
    "v_add_u32 v9, s25, v11\n"
    "s_waitcnt lgkmcnt(2)\n"
    "v_mfma_f32_32x32x16_bf16 v[64:79], v[252:255], v[24:27], v[64:79]\n"
    "ds_read_b128 v[24:27], v2 offset:2048\n"
    "v_xor_b32_e64 v1, 16, v0\n"
    "v_mfma_f32_32x32x16_bf16 v[80:95], v[252:255], v[28:31], v[80:95]\n"
    "s_barrier\n"
    "ds_read_b128 v[28:31], v2 offset:3072\n"
    "s_lshl_b32 s28, s27, 4\n"
    "s_waitcnt lgkmcnt(2)\n"
    "v_mfma_f32_32x32x16_bf16 v[32:47], v[240:243], v[16:19], v[32:47]\n"
    "ds_read_b128 v[16:19], v12 offset:0\n"
    "s_add_u32 s28, s28, %3\n"
    "v_mfma_f32_32x32x16_bf16 v[48:63], v[240:243], v[20:23], v[48:63]\n"
    "ds_read_b128 v[20:23], v13 offset:0\n"
    "s_add_u32 s28, s28, 0xff00\n"
    "s_waitcnt lgkmcnt(2)\n"
    "v_mfma_f32_32x32x16_bf16 v[64:79], v[240:243], v[24:27], v[64:79]\n"
    "ds_read_b128 v[24:27], v14 offset:0\n"
    "v_mov_b32 v6, s28\n"
    "v_mfma_f32_32x32x16_bf16 v[80:95], v[240:243], v[28:31], v[80:95]\n"
    "ds_read_b128 v[28:31], v15 offset:0\n"
    "s_waitcnt lgkmcnt(2)\n"
    "v_mfma_f32_32x32x16_bf16 v[96:111], v[112:115], v[16:19], v[96:111]\n"
    "ds_read_b128 v[16:19], v12 offset:1024\n"
    "v_cvt_pk_bf16_f32 v32, v32, v33\n"
    "v_cvt_pk_bf16_f32 v33, v34, v35\n"
    "v_cvt_pk_bf16_f32 v34, v36, v37\n"
    "v_mfma_f32_32x32x16_bf16 v[96:111], v[116:119], v[20:23], v[96:111]\n"
    "ds_read_b128 v[20:23], v13 offset:1024\n"
    "v_cvt_pk_bf16_f32 v35, v38, v39\n"
    "ds_write_b128 v0, v[32:35] offset:0\n"
    "s_waitcnt lgkmcnt(3)\n"
    "v_mfma_f32_32x32x16_bf16 v[96:111], v[120:123], v[24:27], v[96:111]\n"
    "ds_read_b128 v[24:27], v14 offset:1024\n"
    "v_cvt_pk_bf16_f32 v36, v40, v41\n"
    "v_cvt_pk_bf16_f32 v37, v42, v43\n"
    "v_mfma_f32_32x32x16_bf16 v[96:111], v[124:127], v[28:31], v[96:111]\n"
    "ds_read_b128 v[28:31], v15 offset:1024\n"
    "v_cvt_pk_bf16_f32 v38, v44, v45\n"
    "v_cvt_pk_bf16_f32 v39, v46, v47\n"
    "s_waitcnt lgkmcnt(3)\n"
    "v_mfma_f32_32x32x16_bf16 v[96:111], v[128:131], v[16:19], v[96:111]\n"
    "ds_read_b128 v[16:19], v12 offset:2048\n"
    "ds_write_b128 v1, v[36:39] offset:0\n"
    "ds_write_b32 v6, v6 offset:0\n"
    "v_mfma_f32_32x32x16_bf16 v[96:111], v[132:135], v[20:23], v[96:111]\n"
    "ds_read_b128 v[20:23], v13 offset:2048\n"
    "v_cvt_pk_bf16_f32 v48, v48, v49\n"
    "v_cvt_pk_bf16_f32 v49, v50, v51\n"
    "v_cvt_pk_bf16_f32 v50, v52, v53\n"
    "s_waitcnt lgkmcnt(4)\n"
    "v_mfma_f32_32x32x16_bf16 v[96:111], v[136:139], v[24:27], v[96:111]\n"
    "ds_read_b128 v[24:27], v14 offset:2048\n"
    "v_cvt_pk_bf16_f32 v51, v54, v55\n"
    "ds_write_b128 v0, v[48:51] offset:8192\n"
    "v_mfma_f32_32x32x16_bf16 v[96:111], v[140:143], v[28:31], v[96:111]\n"
    "ds_read_b128 v[28:31], v15 offset:2048\n"
    "v_cvt_pk_bf16_f32 v52, v56, v57\n"
    "v_cvt_pk_bf16_f32 v53, v58, v59\n"
    "s_waitcnt lgkmcnt(3)\n"
    "v_mfma_f32_32x32x16_bf16 v[96:111], v[144:147], v[16:19], v[96:111]\n"
    "ds_read_b128 v[16:19], v12 offset:3072\n"
    "v_cvt_pk_bf16_f32 v54, v60, v61\n"
    "v_cvt_pk_bf16_f32 v55, v62, v63\n"
    "v_mfma_f32_32x32x16_bf16 v[96:111], v[148:151], v[20:23], v[96:111]\n"
    "ds_read_b128 v[20:23], v13 offset:3072\n"
    "ds_write_b128 v1, v[52:55] offset:8192\n"
    "ds_write_b32 v6, v6 offset:4\n"
    "s_waitcnt lgkmcnt(4)\n"
    "v_mfma_f32_32x32x16_bf16 v[96:111], v[152:155], v[24:27], v[96:111]\n"
    "ds_read_b128 v[24:27], v14 offset:3072\n"
    "v_cvt_pk_bf16_f32 v64, v64, v65\n"
    "v_cvt_pk_bf16_f32 v65, v66, v67\n"
    "v_cvt_pk_bf16_f32 v66, v68, v69\n"
    "v_mfma_f32_32x32x16_bf16 v[96:111], v[156:159], v[28:31], v[96:111]\n"
    "ds_read_b128 v[28:31], v15 offset:3072\n"
    "v_cvt_pk_bf16_f32 v67, v70, v71\n"
    "ds_write_b128 v0, v[64:67] offset:16384\n"
    "s_waitcnt lgkmcnt(5)\n"
    "v_mfma_f32_32x32x16_bf16 v[96:111], v[160:163], v[16:19], v[96:111]\n"
    "ds_read_b128 v[16:19], v2 offset:16384\n"
    "v_cvt_pk_bf16_f32 v68, v72, v73\n"
    "v_cvt_pk_bf16_f32 v69, v74, v75\n"
    "v_mfma_f32_32x32x16_bf16 v[96:111], v[164:167], v[20:23], v[96:111]\n"
    "ds_read_b128 v[20:23], v3 offset:16384\n"
    "v_cvt_pk_bf16_f32 v70, v76, v77\n"
    "v_cvt_pk_bf16_f32 v71, v78, v79\n"
    "s_waitcnt lgkmcnt(3)\n"
    "v_mfma_f32_32x32x16_bf16 v[96:111], v[168:171], v[24:27], v[96:111]\n"
    "ds_read_b128 v[24:27], v4 offset:16384\n"
    "ds_write_b128 v1, v[68:71] offset:16384\n"
    "ds_write_b32 v6, v6 offset:8\n"
    "v_mfma_f32_32x32x16_bf16 v[96:111], v[172:175], v[28:31], v[96:111]\n"
    "ds_read_b128 v[28:31], v5 offset:16384\n"
    "v_cvt_pk_bf16_f32 v80, v80, v81\n"
    "v_cvt_pk_bf16_f32 v81, v82, v83\n"
    "v_cvt_pk_bf16_f32 v82, v84, v85\n"
    "s_waitcnt lgkmcnt(4)\n"
    "v_mfma_f32_32x32x16_bf16 v[96:111], v[176:179], v[16:19], v[96:111]\n"
    "ds_read_b128 v[16:19], v2 offset:17408\n"
    "v_cvt_pk_bf16_f32 v83, v86, v87\n"
    "ds_write_b128 v0, v[80:83] offset:24576\n"
    "v_mfma_f32_32x32x16_bf16 v[96:111], v[180:183], v[20:23], v[96:111]\n"
    "ds_read_b128 v[20:23], v3 offset:17408\n"
    "v_cvt_pk_bf16_f32 v84, v88, v89\n"
    "v_cvt_pk_bf16_f32 v85, v90, v91\n"
    "s_waitcnt lgkmcnt(3)\n"
    "v_mfma_f32_32x32x16_bf16 v[96:111], v[184:187], v[24:27], v[96:111]\n"
    "ds_read_b128 v[24:27], v4 offset:17408\n"
    "v_cvt_pk_bf16_f32 v86, v92, v93\n"
    "v_cvt_pk_bf16_f32 v87, v94, v95\n"
    "v_mfma_f32_32x32x16_bf16 v[96:111], v[188:191], v[28:31], v[96:111]\n"
    "ds_read_b128 v[28:31], v5 offset:17408\n"
    "ds_write_b128 v1, v[84:87] offset:24576\n"
    "ds_write_b32 v6, v6 offset:12\n"
    "s_waitcnt lgkmcnt(4)\n"
    "v_mfma_f32_32x32x16_bf16 v[96:111], v[192:195], v[16:19], v[96:111]\n"
    "ds_read_b128 v[16:19], v2 offset:18432\n"
    "v_mfma_f32_32x32x16_bf16 v[96:111], v[196:199], v[20:23], v[96:111]\n"
    "ds_read_b128 v[20:23], v3 offset:18432\n"
    "s_waitcnt lgkmcnt(4)\n"
    "v_mfma_f32_32x32x16_bf16 v[96:111], v[200:203], v[24:27], v[96:111]\n"
    "ds_read_b128 v[24:27], v4 offset:18432\n"
    "v_mfma_f32_32x32x16_bf16 v[96:111], v[204:207], v[28:31], v[96:111]\n"
    "ds_read_b128 v[28:31], v5 offset:18432\n"
    "s_waitcnt lgkmcnt(2)\n"
    "v_mfma_f32_32x32x16_bf16 v[96:111], v[208:211], v[16:19], v[96:111]\n"
    "ds_read_b128 v[16:19], v2 offset:19456\n"
    "v_mfma_f32_32x32x16_bf16 v[96:111], v[212:215], v[20:23], v[96:111]\n"
    "ds_read_b128 v[20:23], v3 offset:19456\n"
    "s_waitcnt lgkmcnt(2)\n"
    "v_mfma_f32_32x32x16_bf16 v[96:111], v[216:219], v[24:27], v[96:111]\n"
    "ds_read_b128 v[24:27], v4 offset:19456\n"
    "v_mfma_f32_32x32x16_bf16 v[96:111], v[220:223], v[28:31], v[96:111]\n"
    "ds_read_b128 v[28:31], v5 offset:19456\n"
    "s_waitcnt lgkmcnt(2)\n"
    "v_mfma_f32_32x32x16_bf16 v[96:111], v[224:227], v[16:19], v[96:111]\n"
    "ds_read_b128 v[16:19], v2 offset:32768\n"
    "v_mfma_f32_32x32x16_bf16 v[96:111], v[228:231], v[20:23], v[96:111]\n"
    "ds_read_b128 v[20:23], v3 offset:32768\n"
    "s_waitcnt lgkmcnt(2)\n"
    "v_mfma_f32_32x32x16_bf16 v[96:111], v[232:235], v[24:27], v[96:111]\n"
    "ds_read_b128 v[24:27], v4 offset:32768\n"
    "v_mfma_f32_32x32x16_bf16 v[96:111], v[236:239], v[28:31], v[96:111]\n"
    "ds_read_b128 v[28:31], v5 offset:32768\n"
    "s_waitcnt lgkmcnt(2)\n"
    "v_mfma_f32_32x32x16_bf16 v[96:111], v[240:243], v[16:19], v[96:111]\n"
    "v_mfma_f32_32x32x16_bf16 v[96:111], v[244:247], v[20:23], v[96:111]\n"
    "s_waitcnt lgkmcnt(1)\n"
    "v_mfma_f32_32x32x16_bf16 v[96:111], v[248:251], v[24:27], v[96:111]\n"
    "s_waitcnt lgkmcnt(0)\n"
    "v_mfma_f32_32x32x16_bf16 v[96:111], v[252:255], v[28:31], v[96:111]\n"
    "s_nop 11\n"
    "v_cvt_pk_bf16_f32 v96, v96, v97\n"
    "v_cvt_pk_bf16_f32 v97, v98, v99\n"
    "v_cvt_pk_bf16_f32 v98, v100, v101\n"
    "v_cvt_pk_bf16_f32 v99, v102, v103\n"
    "ds_write_b128 v0, v[96:99] offset:32768\n"
    "v_cvt_pk_bf16_f32 v100, v104, v105\n"
    "v_cvt_pk_bf16_f32 v101, v106, v107\n"
    "v_cvt_pk_bf16_f32 v102, v108, v109\n"
    "v_cvt_pk_bf16_f32 v103, v110, v111\n"
    "ds_write_b128 v1, v[100:103] offset:32768\n"
    "ds_read_b128 v[104:107], v8 offset:32768\n"
    "ds_read_b128 v[108:111], v8 offset:33792\n"
    "s_add_u32 s28, s36, 0x24000\n"
    "s_waitcnt lgkmcnt(1)\n"
    "buffer_store_dwordx4 v[104:107], v9, s[16:19], s28 offen nt\n"
    "s_add_u32 s28, s36, 0x28800\n"
    "s_waitcnt lgkmcnt(0)\n"
    "buffer_store_dwordx4 v[108:111], v9, s[16:19], s28 offen nt\n"
    "s_branch .Lend%=\n"
    ".p2align 3\n"
    ".Lcons_b%=:\n"
    "s_and_b32 s28, s27, 1\n"
    "s_lshl_b32 s28, s28, 6\n"
    "s_add_u32 s25, s25, s28\n"
    "s_lshr_b32 s28, s27, 1\n"
    "s_mul_i32 s36, s28, 0x1b000\n"
    "s_add_u32 s36, s36, s24\n"
    "s_waitcnt lgkmcnt(0)\n"
    "s_mov_b32 s16, s8\n"
    "s_and_b32 s17, s9, 0xffff\n"
    "buffer_load_dwordx4 v[232:235], v10, s[16:19], s25 offen\n"
    "buffer_load_dwordx4 v[236:239], v10, s[16:19], s25 offen offset:16\n"
    "v_mul_u32_u24 v12, 0x1800, v12\n"
    "s_lshr_b32 s28, s27, 1\n"
    "s_mul_i32 s28, s28, 3072\n"
    "s_add_u32 s28, s28, %3\n"
    "s_add_u32 s28, s28, 0xa000\n"
    "v_add3_u32 v2, v11, v12, s28\n"
    "v_xor_b32 v3, 32, v2\n"
    "v_xor_b32_e64 v4, 64, v2\n"
    "v_xor_b32 v5, 96, v2\n"
    "v_lshrrev_b32 v10, 3, v1\n"
    "v_xor_b32 v11, s27, v10\n"
    "v_and_b32_e64 v11, 1, v11\n"
    "v_bfe_u32 v10, v1, 2, 2\n"
    "v_lshlrev_b32_e64 v11, 6, v11\n"
    "v_lshl_add_u32 v11, v10, 7, v11\n"
    "v_lshrrev_b32_e64 v10, 5, v1\n"
    "v_lshl_add_u32 v11, v10, 10, v11\n"
    "v_and_b32_e64 v10, 1, v1\n"
    "v_lshl_add_u32 v11, v10, 5, v11\n"
    "v_and_b32 v10, 16, v1\n"
    "v_add_u32 v11, v11, v10\n"
    "v_bfe_u32 v10, v1, 1, 1\n"
    "v_lshl_add_u32 v11, v10, 3, v11\n"
    "v_add_u32_e64 v6, %3, v11\n"
    "v_add_u32 v240, 0xc000, v2\n"
    "v_add_u32 v241, 0xc000, v3\n"
    "v_add_u32 v242, 0xc000, v4\n"
    "v_add_u32 v243, 0xc000, v5\n"
    "v_add_u32 v244, 0x18000, v2\n"
    "v_add_u32 v245, 0x18000, v3\n"
    "v_add_u32 v246, 0x18000, v4\n"
    "v_add_u32 v247, 0x18000, v5\n"
    "s_lshl_b32 s30, s27, 11\n"
    "s_add_u32 s30, s30, %3\n"
    "v_lshrrev_b32 v10, 4, v1\n"
    "v_and_b32 v10, 2, v10\n"
    "v_bfe_u32 v11, v1, 1, 2\n"
    "v_xor_b32 v10, v10, v11\n"
    "v_lshlrev_b32 v10, 4, v10\n"
    "v_and_b32_e64 v11, 31, v1\n"
    "v_lshl_add_u32 v10, v11, 6, v10\n"
    "v_add_u32 v0, s30, v10\n"
    "s_barrier\n"
    "ds_read_b64_tr_b16 v[116:117], v6 offset:2048\n"
    "ds_read_b64_tr_b16 v[118:119], v6 offset:2560\n"
    "ds_read_b64_tr_b16 v[120:121], v6 offset:4096\n"
    "ds_read_b64_tr_b16 v[122:123], v6 offset:4608\n"
    "ds_read_b64_tr_b16 v[124:125], v6 offset:6144\n"
    "ds_read_b64_tr_b16 v[126:127], v6 offset:6656\n"
    "s_waitcnt lgkmcnt(4)\n"
    "v_and_b32 v8, s39, v116\n"
    "v_and_b32 v9, s39, v117\n"
    "v_and_b32 v10, s39, v118\n"
    "v_and_b32_e64 v11, s39, v119\n"
    "v_pk_max_u16 v12, v8, v9\n"
    "v_pk_max_u16 v13, v10, v11\n"
    "v_pk_min_u16 v14, v8, v9\n"
    "v_pk_min_u16 v15, v10, v11\n"
    "v_pk_max_u16 v12, v12, v14 op_sel:[0,1] op_sel_hi:[1,0]\n"
    "v_pk_max_u16 v13, v13, v15 op_sel:[0,1] op_sel_hi:[1,0]\n"
    "v_pk_min_u16 v12, v12, v12 op_sel:[0,1] op_sel_hi:[1,0]\n"
    "v_pk_min_u16 v13, v13, v13 op_sel:[0,1] op_sel_hi:[1,0]\n"
    "v_pk_sub_i16 v8, v8, v12\n"
    "v_pk_sub_i16 v9, v9, v12\n"
    "v_pk_sub_i16 v10, v10, v13\n"
    "v_pk_sub_i16 v11, v11, v13\n"
    "v_pk_ashrrev_i16 v8, 15, v8 op_sel_hi:[0,1]\n"
    "v_pk_ashrrev_i16 v9, 15, v9 op_sel_hi:[0,1]\n"
    "v_pk_ashrrev_i16 v10, 15, v10 op_sel_hi:[0,1]\n"
    "v_pk_ashrrev_i16 v11, 15, v11 op_sel_hi:[0,1]\n"
    "v_bitop3_b32 v116, v116, v8, v116 bitop3:0x30\n"
    "v_bitop3_b32 v117, v117, v9, v117 bitop3:0x30\n"
    "v_bitop3_b32 v118, v118, v10, v118 bitop3:0x30\n"
    "v_bitop3_b32 v119, v119, v11, v119 bitop3:0x30\n"
    "s_waitcnt lgkmcnt(2)\n"
    "v_and_b32 v8, s39, v120\n"
    "v_and_b32 v9, s39, v121\n"
    "v_and_b32 v10, s39, v122\n"
    "v_and_b32_e64 v11, s39, v123\n"
    "v_pk_max_u16 v12, v8, v9\n"
    "v_pk_max_u16 v13, v10, v11\n"
    "v_pk_min_u16 v14, v8, v9\n"
    "v_pk_min_u16 v15, v10, v11\n"
    "v_pk_max_u16 v12, v12, v14 op_sel:[0,1] op_sel_hi:[1,0]\n"
    "v_pk_max_u16 v13, v13, v15 op_sel:[0,1] op_sel_hi:[1,0]\n"
    "v_pk_min_u16 v12, v12, v12 op_sel:[0,1] op_sel_hi:[1,0]\n"
    "v_pk_min_u16 v13, v13, v13 op_sel:[0,1] op_sel_hi:[1,0]\n"
    "v_pk_sub_i16 v8, v8, v12\n"
    "v_pk_sub_i16 v9, v9, v12\n"
    "v_pk_sub_i16 v10, v10, v13\n"
    "v_pk_sub_i16 v11, v11, v13\n"
    "v_pk_ashrrev_i16 v8, 15, v8 op_sel_hi:[0,1]\n"
    "v_pk_ashrrev_i16 v9, 15, v9 op_sel_hi:[0,1]\n"
    "v_pk_ashrrev_i16 v10, 15, v10 op_sel_hi:[0,1]\n"
    "v_pk_ashrrev_i16 v11, 15, v11 op_sel_hi:[0,1]\n"
    "v_bitop3_b32 v120, v120, v8, v120 bitop3:0x30\n"
    "v_bitop3_b32 v121, v121, v9, v121 bitop3:0x30\n"
    "v_bitop3_b32 v122, v122, v10, v122 bitop3:0x30\n"
    "v_bitop3_b32 v123, v123, v11, v123 bitop3:0x30\n"
    "s_waitcnt lgkmcnt(0)\n"
    "v_and_b32 v8, s39, v124\n"
    "v_and_b32 v9, s39, v125\n"
    "v_and_b32 v10, s39, v126\n"
    "v_and_b32_e64 v11, s39, v127\n"
    "v_pk_max_u16 v12, v8, v9\n"
    "v_pk_max_u16 v13, v10, v11\n"
    "v_pk_min_u16 v14, v8, v9\n"
    "v_pk_min_u16 v15, v10, v11\n"
    "v_pk_max_u16 v12, v12, v14 op_sel:[0,1] op_sel_hi:[1,0]\n"
    "v_pk_max_u16 v13, v13, v15 op_sel:[0,1] op_sel_hi:[1,0]\n"
    "v_pk_min_u16 v12, v12, v12 op_sel:[0,1] op_sel_hi:[1,0]\n"
    "v_pk_min_u16 v13, v13, v13 op_sel:[0,1] op_sel_hi:[1,0]\n"
    "v_pk_sub_i16 v8, v8, v12\n"
    "v_pk_sub_i16 v9, v9, v12\n"
    "v_pk_sub_i16 v10, v10, v13\n"
    "v_pk_sub_i16 v11, v11, v13\n"
    "v_pk_ashrrev_i16 v8, 15, v8 op_sel_hi:[0,1]\n"
    "v_pk_ashrrev_i16 v9, 15, v9 op_sel_hi:[0,1]\n"
    "v_pk_ashrrev_i16 v10, 15, v10 op_sel_hi:[0,1]\n"
    "v_pk_ashrrev_i16 v11, 15, v11 op_sel_hi:[0,1]\n"
    "v_bitop3_b32 v124, v124, v8, v124 bitop3:0x30\n"
    "v_bitop3_b32 v125, v125, v9, v125 bitop3:0x30\n"
    "v_bitop3_b32 v126, v126, v10, v126 bitop3:0x30\n"
    "v_bitop3_b32 v127, v127, v11, v127 bitop3:0x30\n"
    "s_waitcnt vmcnt(0)\n"
    "v_and_b32 v81, 0xffff0000, v232\n"
    "v_lshlrev_b32 v80, 16, v232\n"
    "v_and_b32 v83, 0xffff0000, v233\n"
    "v_lshlrev_b32_e64 v82, 16, v233\n"
    "v_and_b32 v85, 0xffff0000, v234\n"
    "v_lshlrev_b32_e64 v84, 16, v234\n"
    "v_and_b32 v87, 0xffff0000, v235\n"
    "v_lshlrev_b32_e64 v86, 16, v235\n"
    "v_and_b32 v89, 0xffff0000, v236\n"
    "v_lshlrev_b32_e64 v88, 16, v236\n"
    "v_and_b32 v91, 0xffff0000, v237\n"
    "v_lshlrev_b32_e64 v90, 16, v237\n"
    "v_and_b32 v93, 0xffff0000, v238\n"
    "v_lshlrev_b32_e64 v92, 16, v238\n"
    "v_and_b32 v95, 0xffff0000, v239\n"
    "v_lshlrev_b32 v94, 16, v239\n"
    "s_mov_b32 s16, s10\n"
    "s_and_b32 s17, s11, 0xffff\n"
    "s_mov_b32 s18, 0x900000\n"
    "s_barrier\n"
    "ds_read_b128 v[112:115], v7 offset:0\n"
    "ds_read_b128 v[16:19], v3 offset:0\n"
    "ds_read_b128 v[20:23], v3 offset:1024\n"
    "ds_read_b128 v[24:27], v3 offset:2048\n"
    "ds_read_b128 v[28:31], v4 offset:0\n"
    "ds_read_b64_tr_b16 v[132:133], v6 offset:10240\n"
    "ds_read_b64_tr_b16 v[134:135], v6 offset:10752\n"
    "s_waitcnt lgkmcnt(4)\n"
    "v_mfma_f32_32x32x16_bf16 v[32:47], v[116:119], v[16:19], v[80:95]\n"
    "ds_read_b128 v[16:19], v4 offset:1024\n"
    "v_mfma_f32_32x32x16_bf16 v[48:63], v[116:119], v[20:23], v[80:95]\n"
    "ds_read_b128 v[20:23], v4 offset:2048\n"
    "s_waitcnt lgkmcnt(2)\n"
    "v_and_b32 v8, s39, v132\n"
    "v_and_b32 v9, s39, v133\n"
    "v_and_b32 v10, s39, v134\n"
    "v_and_b32_e64 v11, s39, v135\n"
    "v_pk_max_u16 v12, v8, v9\n"
    "v_pk_max_u16 v13, v10, v11\n"
    "v_pk_min_u16 v14, v8, v9\n"
    "v_pk_min_u16 v15, v10, v11\n"
    "v_pk_max_u16 v12, v12, v14 op_sel:[0,1] op_sel_hi:[1,0]\n"
    "v_pk_max_u16 v13, v13, v15 op_sel:[0,1] op_sel_hi:[1,0]\n"
    "v_mfma_f32_32x32x16_bf16 v[64:79], v[116:119], v[24:27], v[80:95]\n"
    "ds_read_b128 v[24:27], v5 offset:0\n"
    "v_pk_min_u16 v12, v12, v12 op_sel:[0,1] op_sel_hi:[1,0]\n"
    "v_pk_min_u16 v13, v13, v13 op_sel:[0,1] op_sel_hi:[1,0]\n"
    "v_pk_sub_i16 v8, v8, v12\n"
    "v_pk_sub_i16 v9, v9, v12\n"
    "v_pk_sub_i16 v10, v10, v13\n"
    "v_pk_sub_i16 v11, v11, v13\n"
    "ds_read_b64_tr_b16 v[136:137], v6 offset:12288\n"
    "ds_read_b64_tr_b16 v[138:139], v6 offset:12800\n"
    "v_pk_ashrrev_i16 v8, 15, v8 op_sel_hi:[0,1]\n"
    "v_pk_ashrrev_i16 v9, 15, v9 op_sel_hi:[0,1]\n"
    "v_pk_ashrrev_i16 v10, 15, v10 op_sel_hi:[0,1]\n"
    "v_pk_ashrrev_i16 v11, 15, v11 op_sel_hi:[0,1]\n"
    "v_mfma_f32_32x32x16_bf16 v[32:47], v[120:123], v[28:31], v[32:47]\n"
    "ds_read_b128 v[28:31], v5 offset:1024\n"
    "v_bitop3_b32 v132, v132, v8, v132 bitop3:0x30\n"
    "v_bitop3_b32 v133, v133, v9, v133 bitop3:0x30\n"
    "v_bitop3_b32 v134, v134, v10, v134 bitop3:0x30\n"
    "v_bitop3_b32 v135, v135, v11, v135 bitop3:0x30\n"
    "s_waitcnt lgkmcnt(1)\n"
    "v_and_b32 v8, s39, v136\n"
    "v_and_b32 v9, s39, v137\n"
    "v_and_b32 v10, s39, v138\n"
    "v_and_b32_e64 v11, s39, v139\n"
    "v_pk_max_u16 v12, v8, v9\n"
    "v_pk_max_u16 v13, v10, v11\n"
    "v_mfma_f32_32x32x16_bf16 v[48:63], v[120:123], v[16:19], v[48:63]\n"
    "ds_read_b128 v[16:19], v5 offset:2048\n"
    "v_pk_min_u16 v14, v8, v9\n"
    "v_pk_min_u16 v15, v10, v11\n"
    "v_pk_max_u16 v12, v12, v14 op_sel:[0,1] op_sel_hi:[1,0]\n"
    "v_pk_max_u16 v13, v13, v15 op_sel:[0,1] op_sel_hi:[1,0]\n"
    "v_pk_min_u16 v12, v12, v12 op_sel:[0,1] op_sel_hi:[1,0]\n"
    "v_pk_min_u16 v13, v13, v13 op_sel:[0,1] op_sel_hi:[1,0]\n"
    "v_pk_sub_i16 v8, v8, v12\n"
    "v_pk_sub_i16 v9, v9, v12\n"
    "v_pk_sub_i16 v10, v10, v13\n"
    "v_pk_sub_i16 v11, v11, v13\n"
    "ds_read_b64_tr_b16 v[140:141], v6 offset:14336\n"
    "ds_read_b64_tr_b16 v[142:143], v6 offset:14848\n"
    "v_mfma_f32_32x32x16_bf16 v[64:79], v[120:123], v[20:23], v[64:79]\n"
    "ds_read_b128 v[20:23], v2 offset:0\n"
    "v_pk_ashrrev_i16 v8, 15, v8 op_sel_hi:[0,1]\n"
    "v_pk_ashrrev_i16 v9, 15, v9 op_sel_hi:[0,1]\n"
    "v_pk_ashrrev_i16 v10, 15, v10 op_sel_hi:[0,1]\n"
    "v_pk_ashrrev_i16 v11, 15, v11 op_sel_hi:[0,1]\n"
    "v_bitop3_b32 v136, v136, v8, v136 bitop3:0x30\n"
    "v_bitop3_b32 v137, v137, v9, v137 bitop3:0x30\n"
    "v_bitop3_b32 v138, v138, v10, v138 bitop3:0x30\n"
    "v_bitop3_b32 v139, v139, v11, v139 bitop3:0x30\n"
    "s_waitcnt lgkmcnt(1)\n"
    "v_and_b32 v8, s39, v140\n"
    "v_and_b32_e64 v9, s39, v141\n"
    "v_mfma_f32_32x32x16_bf16 v[32:47], v[124:127], v[24:27], v[32:47]\n"
    "ds_read_b128 v[24:27], v2 offset:1024\n"
    "v_and_b32 v10, s39, v142\n"
    "v_and_b32 v11, s39, v143\n"
    "v_pk_max_u16 v12, v8, v9\n"
    "v_pk_max_u16 v13, v10, v11\n"
    "v_pk_min_u16 v14, v8, v9\n"
    "v_pk_min_u16 v15, v10, v11\n"
    "v_pk_max_u16 v12, v12, v14 op_sel:[0,1] op_sel_hi:[1,0]\n"
    "v_pk_max_u16 v13, v13, v15 op_sel:[0,1] op_sel_hi:[1,0]\n"
    "v_pk_min_u16 v12, v12, v12 op_sel:[0,1] op_sel_hi:[1,0]\n"
    "v_pk_min_u16 v13, v13, v13 op_sel:[0,1] op_sel_hi:[1,0]\n"
    "v_pk_sub_i16 v8, v8, v12\n"
    "v_mfma_f32_32x32x16_bf16 v[48:63], v[124:127], v[28:31], v[48:63]\n"
    "s_barrier\n"
    "ds_read_b128 v[128:131], v7 offset:4096\n"
    "ds_read_b128 v[28:31], v2 offset:2048\n"
    "v_pk_sub_i16 v9, v9, v12\n"
    "v_pk_sub_i16 v10, v10, v13\n"
    "v_pk_sub_i16 v11, v11, v13\n"
    "v_pk_ashrrev_i16 v8, 15, v8 op_sel_hi:[0,1]\n"
    "v_pk_ashrrev_i16 v9, 15, v9 op_sel_hi:[0,1]\n"
    "v_pk_ashrrev_i16 v10, 15, v10 op_sel_hi:[0,1]\n"
    "v_pk_ashrrev_i16 v11, 15, v11 op_sel_hi:[0,1]\n"
    "v_bitop3_b32 v140, v140, v8, v140 bitop3:0x30\n"
    "v_bitop3_b32 v141, v141, v9, v141 bitop3:0x30\n"
    "v_bitop3_b32 v142, v142, v10, v142 bitop3:0x30\n"
    "v_bitop3_b32 v143, v143, v11, v143 bitop3:0x30\n"
    "ds_read_b64_tr_b16 v[148:149], v6 offset:18432\n"
    "ds_read_b64_tr_b16 v[150:151], v6 offset:18944\n"
    "v_mfma_f32_32x32x16_bf16 v[64:79], v[124:127], v[16:19], v[64:79]\n"
    "ds_read_b128 v[16:19], v3 offset:24576\n"
    "s_waitcnt lgkmcnt(5)\n"
    "v_mfma_f32_32x32x16_bf16 v[32:47], v[112:115], v[20:23], v[32:47]\n"
    "ds_read_b128 v[20:23], v3 offset:25600\n"
    "v_mfma_f32_32x32x16_bf16 v[48:63], v[112:115], v[24:27], v[48:63]\n"
    "ds_read_b128 v[24:27], v3 offset:26624\n"
    "s_waitcnt lgkmcnt(3)\n"
    "v_and_b32 v8, s39, v148\n"
    "v_and_b32 v9, s39, v149\n"
    "v_and_b32 v10, s39, v150\n"
    "v_and_b32_e64 v11, s39, v151\n"
    "v_pk_max_u16 v12, v8, v9\n"
    "v_pk_max_u16 v13, v10, v11\n"
    "v_pk_min_u16 v14, v8, v9\n"
    "v_mfma_f32_32x32x16_bf16 v[64:79], v[112:115], v[28:31], v[64:79]\n"
    "ds_read_b128 v[28:31], v4 offset:24576\n"
    "v_pk_min_u16 v15, v10, v11\n"
    "v_pk_max_u16 v12, v12, v14 op_sel:[0,1] op_sel_hi:[1,0]\n"
    "v_pk_max_u16 v13, v13, v15 op_sel:[0,1] op_sel_hi:[1,0]\n"
    "v_pk_min_u16 v12, v12, v12 op_sel:[0,1] op_sel_hi:[1,0]\n"
    "v_pk_min_u16 v13, v13, v13 op_sel:[0,1] op_sel_hi:[1,0]\n"
    "v_pk_sub_i16 v8, v8, v12\n"
    "v_pk_sub_i16 v9, v9, v12\n"
    "v_pk_sub_i16 v10, v10, v13\n"
    "s_waitcnt lgkmcnt(2)\n"
    "v_mfma_f32_32x32x16_bf16 v[32:47], v[132:135], v[16:19], v[32:47]\n"
    "ds_read_b128 v[16:19], v4 offset:25600\n"
    "v_pk_sub_i16 v11, v11, v13\n"
    "ds_read_b64_tr_b16 v[152:153], v6 offset:20480\n"
    "ds_read_b64_tr_b16 v[154:155], v6 offset:20992\n"
    "v_pk_ashrrev_i16 v8, 15, v8 op_sel_hi:[0,1]\n"
    "v_pk_ashrrev_i16 v9, 15, v9 op_sel_hi:[0,1]\n"
    "v_pk_ashrrev_i16 v10, 15, v10 op_sel_hi:[0,1]\n"
    "v_pk_ashrrev_i16 v11, 15, v11 op_sel_hi:[0,1]\n"
    "v_bitop3_b32 v148, v148, v8, v148 bitop3:0x30\n"
    "v_bitop3_b32 v149, v149, v9, v149 bitop3:0x30\n"
    "v_mfma_f32_32x32x16_bf16 v[48:63], v[132:135], v[20:23], v[48:63]\n"
    "ds_read_b128 v[20:23], v4 offset:26624\n"
    "v_bitop3_b32 v150, v150, v10, v150 bitop3:0x30\n"
    "v_bitop3_b32 v151, v151, v11, v151 bitop3:0x30\n"
    "s_waitcnt lgkmcnt(1)\n"
    "v_and_b32 v8, s39, v152\n"
    "v_and_b32 v9, s39, v153\n"
    "v_and_b32 v10, s39, v154\n"
    "v_and_b32 v11, s39, v155\n"
    "v_mfma_f32_32x32x16_bf16 v[64:79], v[132:135], v[24:27], v[64:79]\n"
    "ds_read_b128 v[24:27], v5 offset:24576\n"
    "v_pk_max_u16 v12, v8, v9\n"
    "v_pk_max_u16 v13, v10, v11\n"
    "v_pk_min_u16 v14, v8, v9\n"
    "v_pk_min_u16 v15, v10, v11\n"
    "v_pk_max_u16 v12, v12, v14 op_sel:[0,1] op_sel_hi:[1,0]\n"
    "v_pk_max_u16 v13, v13, v15 op_sel:[0,1] op_sel_hi:[1,0]\n"
    "v_pk_min_u16 v12, v12, v12 op_sel:[0,1] op_sel_hi:[1,0]\n"
    "v_pk_min_u16 v13, v13, v13 op_sel:[0,1] op_sel_hi:[1,0]\n"
    "v_mfma_f32_32x32x16_bf16 v[32:47], v[136:139], v[28:31], v[32:47]\n"
    "ds_read_b128 v[28:31], v5 offset:25600\n"
    "v_pk_sub_i16 v8, v8, v12\n"
    "v_pk_sub_i16 v9, v9, v12\n"
    "v_pk_sub_i16 v10, v10, v13\n"
    "v_pk_sub_i16 v11, v11, v13\n"
    "ds_read_b64_tr_b16 v[156:157], v6 offset:22528\n"
    "ds_read_b64_tr_b16 v[158:159], v6 offset:23040\n"
    "v_pk_ashrrev_i16 v8, 15, v8 op_sel_hi:[0,1]\n"
    "v_pk_ashrrev_i16 v9, 15, v9 op_sel_hi:[0,1]\n"
    "v_pk_ashrrev_i16 v10, 15, v10 op_sel_hi:[0,1]\n"
    "v_mfma_f32_32x32x16_bf16 v[48:63], v[136:139], v[16:19], v[48:63]\n"
    "ds_read_b128 v[16:19], v5 offset:26624\n"
    "v_pk_ashrrev_i16 v11, 15, v11 op_sel_hi:[0,1]\n"
    "v_bitop3_b32 v152, v152, v8, v152 bitop3:0x30\n"
    "v_bitop3_b32 v153, v153, v9, v153 bitop3:0x30\n"
    "v_bitop3_b32 v154, v154, v10, v154 bitop3:0x30\n"
    "v_bitop3_b32 v155, v155, v11, v155 bitop3:0x30\n"
    "s_waitcnt lgkmcnt(1)\n"
    "v_and_b32 v8, s39, v156\n"
    "v_mfma_f32_32x32x16_bf16 v[64:79], v[136:139], v[20:23], v[64:79]\n"
    "ds_read_b128 v[20:23], v2 offset:24576\n"
    "v_and_b32 v9, s39, v157\n"
    "v_and_b32 v10, s39, v158\n"
    "v_and_b32_e64 v11, s39, v159\n"
    "v_pk_max_u16 v12, v8, v9\n"
    "v_pk_max_u16 v13, v10, v11\n"
    "v_pk_min_u16 v14, v8, v9\n"
    "v_pk_min_u16 v15, v10, v11\n"
    "v_pk_max_u16 v12, v12, v14 op_sel:[0,1] op_sel_hi:[1,0]\n"
    "v_mfma_f32_32x32x16_bf16 v[32:47], v[140:143], v[24:27], v[32:47]\n"
    "ds_read_b128 v[24:27], v2 offset:25600\n"
    "v_pk_max_u16 v13, v13, v15 op_sel:[0,1] op_sel_hi:[1,0]\n"
    "v_pk_min_u16 v12, v12, v12 op_sel:[0,1] op_sel_hi:[1,0]\n"
    "v_pk_min_u16 v13, v13, v13 op_sel:[0,1] op_sel_hi:[1,0]\n"
    "v_pk_sub_i16 v8, v8, v12\n"
    "v_pk_sub_i16 v9, v9, v12\n"
    "v_pk_sub_i16 v10, v10, v13\n"
    "v_pk_sub_i16 v11, v11, v13\n"
    "v_pk_ashrrev_i16 v8, 15, v8 op_sel_hi:[0,1]\n"
    "v_mfma_f32_32x32x16_bf16 v[48:63], v[140:143], v[28:31], v[48:63]\n"
    "s_barrier\n"
    "ds_read_b128 v[144:147], v7 offset:0\n"
    "ds_read_b128 v[28:31], v2 offset:26624\n"
    "v_pk_ashrrev_i16 v9, 15, v9 op_sel_hi:[0,1]\n"
    "v_pk_ashrrev_i16 v10, 15, v10 op_sel_hi:[0,1]\n"
    "v_pk_ashrrev_i16 v11, 15, v11 op_sel_hi:[0,1]\n"
    "v_bitop3_b32 v156, v156, v8, v156 bitop3:0x30\n"
    "v_bitop3_b32 v157, v157, v9, v157 bitop3:0x30\n"
    "v_bitop3_b32 v158, v158, v10, v158 bitop3:0x30\n"
    "v_bitop3_b32 v159, v159, v11, v159 bitop3:0x30\n"
    "ds_read_b64_tr_b16 v[164:165], v6 offset:26624\n"
    "ds_read_b64_tr_b16 v[166:167], v6 offset:27136\n"
    "s_waitcnt lgkmcnt(5)\n"
    "v_mfma_f32_32x32x16_bf16 v[64:79], v[140:143], v[16:19], v[64:79]\n"
    "ds_read_b128 v[16:19], v241 offset:0\n"
    "v_mfma_f32_32x32x16_bf16 v[32:47], v[128:131], v[20:23], v[32:47]\n"
    "ds_read_b128 v[20:23], v241 offset:1024\n"
    "s_waitcnt lgkmcnt(4)\n"
    "v_mfma_f32_32x32x16_bf16 v[48:63], v[128:131], v[24:27], v[48:63]\n"
    "ds_read_b128 v[24:27], v241 offset:2048\n"
    "s_waitcnt lgkmcnt(3)\n"
    "v_and_b32 v8, s39, v164\n"
    "v_and_b32 v9, s39, v165\n"
    "v_and_b32 v10, s39, v166\n"
    "v_and_b32 v11, s39, v167\n"
    "v_pk_max_u16 v12, v8, v9\n"
    "v_pk_max_u16 v13, v10, v11\n"
    "v_pk_min_u16 v14, v8, v9\n"
    "v_mfma_f32_32x32x16_bf16 v[64:79], v[128:131], v[28:31], v[64:79]\n"
    "ds_read_b128 v[28:31], v242 offset:0\n"
    "v_pk_min_u16 v15, v10, v11\n"
    "v_pk_max_u16 v12, v12, v14 op_sel:[0,1] op_sel_hi:[1,0]\n"
    "v_pk_max_u16 v13, v13, v15 op_sel:[0,1] op_sel_hi:[1,0]\n"
    "v_pk_min_u16 v12, v12, v12 op_sel:[0,1] op_sel_hi:[1,0]\n"
    "v_pk_min_u16 v13, v13, v13 op_sel:[0,1] op_sel_hi:[1,0]\n"
    "v_pk_sub_i16 v8, v8, v12\n"
    "v_pk_sub_i16 v9, v9, v12\n"
    "v_pk_sub_i16 v10, v10, v13\n"
    "s_waitcnt lgkmcnt(2)\n"
    "v_mfma_f32_32x32x16_bf16 v[32:47], v[148:151], v[16:19], v[32:47]\n"
    "ds_read_b128 v[16:19], v242 offset:1024\n"
    "v_pk_sub_i16 v11, v11, v13\n"
    "ds_read_b64_tr_b16 v[168:169], v6 offset:28672\n"
    "ds_read_b64_tr_b16 v[170:171], v6 offset:29184\n"
    "v_pk_ashrrev_i16 v8, 15, v8 op_sel_hi:[0,1]\n"
    "v_pk_ashrrev_i16 v9, 15, v9 op_sel_hi:[0,1]\n"
    "v_pk_ashrrev_i16 v10, 15, v10 op_sel_hi:[0,1]\n"
    "v_pk_ashrrev_i16 v11, 15, v11 op_sel_hi:[0,1]\n"
    "v_bitop3_b32 v164, v164, v8, v164 bitop3:0x30\n"
    "v_bitop3_b32 v165, v165, v9, v165 bitop3:0x30\n"
    "v_mfma_f32_32x32x16_bf16 v[48:63], v[148:151], v[20:23], v[48:63]\n"
    "ds_read_b128 v[20:23], v242 offset:2048\n"
    "v_bitop3_b32 v166, v166, v10, v166 bitop3:0x30\n"
    "v_bitop3_b32 v167, v167, v11, v167 bitop3:0x30\n"
    "s_waitcnt lgkmcnt(1)\n"
    "v_and_b32 v8, s39, v168\n"
    "v_and_b32 v9, s39, v169\n"
    "v_and_b32 v10, s39, v170\n"
    "v_and_b32 v11, s39, v171\n"
    "v_mfma_f32_32x32x16_bf16 v[64:79], v[148:151], v[24:27], v[64:79]\n"
    "ds_read_b128 v[24:27], v243 offset:0\n"
    "v_pk_max_u16 v12, v8, v9\n"
    "v_pk_max_u16 v13, v10, v11\n"
    "v_pk_min_u16 v14, v8, v9\n"
    "v_pk_min_u16 v15, v10, v11\n"
    "v_pk_max_u16 v12, v12, v14 op_sel:[0,1] op_sel_hi:[1,0]\n"
    "v_pk_max_u16 v13, v13, v15 op_sel:[0,1] op_sel_hi:[1,0]\n"
    "v_pk_min_u16 v12, v12, v12 op_sel:[0,1] op_sel_hi:[1,0]\n"
    "v_pk_min_u16 v13, v13, v13 op_sel:[0,1] op_sel_hi:[1,0]\n"
    "v_mfma_f32_32x32x16_bf16 v[32:47], v[152:155], v[28:31], v[32:47]\n"
    "ds_read_b128 v[28:31], v243 offset:1024\n"
    "v_pk_sub_i16 v8, v8, v12\n"
    "v_pk_sub_i16 v9, v9, v12\n"
    "v_pk_sub_i16 v10, v10, v13\n"
    "v_pk_sub_i16 v11, v11, v13\n"
    "ds_read_b64_tr_b16 v[172:173], v6 offset:30720\n"
    "ds_read_b64_tr_b16 v[174:175], v6 offset:31232\n"
    "v_pk_ashrrev_i16 v8, 15, v8 op_sel_hi:[0,1]\n"
    "v_pk_ashrrev_i16 v9, 15, v9 op_sel_hi:[0,1]\n"
    "v_pk_ashrrev_i16 v10, 15, v10 op_sel_hi:[0,1]\n"
    "v_mfma_f32_32x32x16_bf16 v[48:63], v[152:155], v[16:19], v[48:63]\n"
    "ds_read_b128 v[16:19], v243 offset:2048\n"
    "v_pk_ashrrev_i16 v11, 15, v11 op_sel_hi:[0,1]\n"
    "v_bitop3_b32 v168, v168, v8, v168 bitop3:0x30\n"
    "v_bitop3_b32 v169, v169, v9, v169 bitop3:0x30\n"
    "v_bitop3_b32 v170, v170, v10, v170 bitop3:0x30\n"
    "v_bitop3_b32 v171, v171, v11, v171 bitop3:0x30\n"
    "s_waitcnt lgkmcnt(1)\n"
    "v_and_b32 v8, s39, v172\n"
    "v_mfma_f32_32x32x16_bf16 v[64:79], v[152:155], v[20:23], v[64:79]\n"
    "ds_read_b128 v[20:23], v240 offset:0\n"
    "v_and_b32 v9, s39, v173\n"
    "v_and_b32 v10, s39, v174\n"
    "v_and_b32_e64 v11, s39, v175\n"
    "v_pk_max_u16 v12, v8, v9\n"
    "v_pk_max_u16 v13, v10, v11\n"
    "v_pk_min_u16 v14, v8, v9\n"
    "v_pk_min_u16 v15, v10, v11\n"
    "v_pk_max_u16 v12, v12, v14 op_sel:[0,1] op_sel_hi:[1,0]\n"
    "v_mfma_f32_32x32x16_bf16 v[32:47], v[156:159], v[24:27], v[32:47]\n"
    "ds_read_b128 v[24:27], v240 offset:1024\n"
    "v_pk_max_u16 v13, v13, v15 op_sel:[0,1] op_sel_hi:[1,0]\n"
    "v_pk_min_u16 v12, v12, v12 op_sel:[0,1] op_sel_hi:[1,0]\n"
    "v_pk_min_u16 v13, v13, v13 op_sel:[0,1] op_sel_hi:[1,0]\n"
    "v_pk_sub_i16 v8, v8, v12\n"
    "v_pk_sub_i16 v9, v9, v12\n"
    "v_pk_sub_i16 v10, v10, v13\n"
    "v_pk_sub_i16 v11, v11, v13\n"
    "v_pk_ashrrev_i16 v8, 15, v8 op_sel_hi:[0,1]\n"
    "v_mfma_f32_32x32x16_bf16 v[48:63], v[156:159], v[28:31], v[48:63]\n"
    "s_barrier\n"
    "ds_read_b128 v[160:163], v7 offset:4096\n"
    "ds_read_b128 v[28:31], v240 offset:2048\n"
    "v_pk_ashrrev_i16 v9, 15, v9 op_sel_hi:[0,1]\n"
    "v_pk_ashrrev_i16 v10, 15, v10 op_sel_hi:[0,1]\n"
    "v_pk_ashrrev_i16 v11, 15, v11 op_sel_hi:[0,1]\n"
    "v_bitop3_b32 v172, v172, v8, v172 bitop3:0x30\n"
    "v_bitop3_b32 v173, v173, v9, v173 bitop3:0x30\n"
    "v_bitop3_b32 v174, v174, v10, v174 bitop3:0x30\n"
    "v_bitop3_b32 v175, v175, v11, v175 bitop3:0x30\n"
    "ds_read_b64_tr_b16 v[180:181], v6 offset:2048\n"
    "ds_read_b64_tr_b16 v[182:183], v6 offset:2560\n"
    "s_waitcnt lgkmcnt(5)\n"
    "v_mfma_f32_32x32x16_bf16 v[64:79], v[156:159], v[16:19], v[64:79]\n"
    "ds_read_b128 v[16:19], v241 offset:24576\n"
    "v_mfma_f32_32x32x16_bf16 v[32:47], v[144:147], v[20:23], v[32:47]\n"
    "ds_read_b128 v[20:23], v241 offset:25600\n"
    "s_waitcnt lgkmcnt(4)\n"
    "v_mfma_f32_32x32x16_bf16 v[48:63], v[144:147], v[24:27], v[48:63]\n"
    "ds_read_b128 v[24:27], v241 offset:26624\n"
    "s_waitcnt lgkmcnt(3)\n"
    "v_and_b32 v8, s39, v180\n"
    "v_and_b32 v9, s39, v181\n"
    "v_and_b32 v10, s39, v182\n"
    "v_and_b32 v11, s39, v183\n"
    "v_pk_max_u16 v12, v8, v9\n"
    "v_pk_max_u16 v13, v10, v11\n"
    "v_pk_min_u16 v14, v8, v9\n"
    "v_mfma_f32_32x32x16_bf16 v[64:79], v[144:147], v[28:31], v[64:79]\n"
    "ds_read_b128 v[28:31], v242 offset:24576\n"
    "v_pk_min_u16 v15, v10, v11\n"
    "v_pk_max_u16 v12, v12, v14 op_sel:[0,1] op_sel_hi:[1,0]\n"
    "v_pk_max_u16 v13, v13, v15 op_sel:[0,1] op_sel_hi:[1,0]\n"
    "v_pk_min_u16 v12, v12, v12 op_sel:[0,1] op_sel_hi:[1,0]\n"
    "v_pk_min_u16 v13, v13, v13 op_sel:[0,1] op_sel_hi:[1,0]\n"
    "v_pk_sub_i16 v8, v8, v12\n"
    "v_pk_sub_i16 v9, v9, v12\n"
    "v_pk_sub_i16 v10, v10, v13\n"
    "s_waitcnt lgkmcnt(2)\n"
    "v_mfma_f32_32x32x16_bf16 v[32:47], v[164:167], v[16:19], v[32:47]\n"
    "ds_read_b128 v[16:19], v242 offset:25600\n"
    "v_pk_sub_i16 v11, v11, v13\n"
    "ds_read_b64_tr_b16 v[184:185], v6 offset:4096\n"
    "ds_read_b64_tr_b16 v[186:187], v6 offset:4608\n"
    "v_pk_ashrrev_i16 v8, 15, v8 op_sel_hi:[0,1]\n"
    "v_pk_ashrrev_i16 v9, 15, v9 op_sel_hi:[0,1]\n"
    "v_pk_ashrrev_i16 v10, 15, v10 op_sel_hi:[0,1]\n"
    "v_pk_ashrrev_i16 v11, 15, v11 op_sel_hi:[0,1]\n"
    "v_bitop3_b32 v180, v180, v8, v180 bitop3:0x30\n"
    "v_bitop3_b32 v181, v181, v9, v181 bitop3:0x30\n"
    "v_mfma_f32_32x32x16_bf16 v[48:63], v[164:167], v[20:23], v[48:63]\n"
    "ds_read_b128 v[20:23], v242 offset:26624\n"
    "v_bitop3_b32 v182, v182, v10, v182 bitop3:0x30\n"
    "v_bitop3_b32 v183, v183, v11, v183 bitop3:0x30\n"
    "s_waitcnt lgkmcnt(1)\n"
    "v_and_b32 v8, s39, v184\n"
    "v_and_b32 v9, s39, v185\n"
    "v_and_b32 v10, s39, v186\n"
    "v_and_b32 v11, s39, v187\n"
    "v_mfma_f32_32x32x16_bf16 v[64:79], v[164:167], v[24:27], v[64:79]\n"
    "ds_read_b128 v[24:27], v243 offset:24576\n"
    "v_pk_max_u16 v12, v8, v9\n"
    "v_pk_max_u16 v13, v10, v11\n"
    "v_pk_min_u16 v14, v8, v9\n"
    "v_pk_min_u16 v15, v10, v11\n"
    "v_pk_max_u16 v12, v12, v14 op_sel:[0,1] op_sel_hi:[1,0]\n"
    "v_pk_max_u16 v13, v13, v15 op_sel:[0,1] op_sel_hi:[1,0]\n"
    "v_pk_min_u16 v12, v12, v12 op_sel:[0,1] op_sel_hi:[1,0]\n"
    "v_pk_min_u16 v13, v13, v13 op_sel:[0,1] op_sel_hi:[1,0]\n"
    "v_mfma_f32_32x32x16_bf16 v[32:47], v[168:171], v[28:31], v[32:47]\n"
    "ds_read_b128 v[28:31], v243 offset:25600\n"
    "v_pk_sub_i16 v8, v8, v12\n"
    "v_pk_sub_i16 v9, v9, v12\n"
    "v_pk_sub_i16 v10, v10, v13\n"
    "v_pk_sub_i16 v11, v11, v13\n"
    "ds_read_b64_tr_b16 v[188:189], v6 offset:6144\n"
    "ds_read_b64_tr_b16 v[190:191], v6 offset:6656\n"
    "v_pk_ashrrev_i16 v8, 15, v8 op_sel_hi:[0,1]\n"
    "v_pk_ashrrev_i16 v9, 15, v9 op_sel_hi:[0,1]\n"
    "v_pk_ashrrev_i16 v10, 15, v10 op_sel_hi:[0,1]\n"
    "v_mfma_f32_32x32x16_bf16 v[48:63], v[168:171], v[16:19], v[48:63]\n"
    "ds_read_b128 v[16:19], v243 offset:26624\n"
    "v_pk_ashrrev_i16 v11, 15, v11 op_sel_hi:[0,1]\n"
    "v_bitop3_b32 v184, v184, v8, v184 bitop3:0x30\n"
    "v_bitop3_b32 v185, v185, v9, v185 bitop3:0x30\n"
    "v_bitop3_b32 v186, v186, v10, v186 bitop3:0x30\n"
    "v_bitop3_b32 v187, v187, v11, v187 bitop3:0x30\n"
    "s_waitcnt lgkmcnt(1)\n"
    "v_and_b32 v8, s39, v188\n"
    "v_mfma_f32_32x32x16_bf16 v[64:79], v[168:171], v[20:23], v[64:79]\n"
    "ds_read_b128 v[20:23], v240 offset:24576\n"
    "v_and_b32 v9, s39, v189\n"
    "v_and_b32 v10, s39, v190\n"
    "v_and_b32_e64 v11, s39, v191\n"
    "v_pk_max_u16 v12, v8, v9\n"
    "v_pk_max_u16 v13, v10, v11\n"
    "v_pk_min_u16 v14, v8, v9\n"
    "v_pk_min_u16 v15, v10, v11\n"
    "v_pk_max_u16 v12, v12, v14 op_sel:[0,1] op_sel_hi:[1,0]\n"
    "v_mfma_f32_32x32x16_bf16 v[32:47], v[172:175], v[24:27], v[32:47]\n"
    "ds_read_b128 v[24:27], v240 offset:25600\n"
    "v_pk_max_u16 v13, v13, v15 op_sel:[0,1] op_sel_hi:[1,0]\n"
    "v_pk_min_u16 v12, v12, v12 op_sel:[0,1] op_sel_hi:[1,0]\n"
    "v_pk_min_u16 v13, v13, v13 op_sel:[0,1] op_sel_hi:[1,0]\n"
    "v_pk_sub_i16 v8, v8, v12\n"
    "v_pk_sub_i16 v9, v9, v12\n"
    "v_pk_sub_i16 v10, v10, v13\n"
    "v_pk_sub_i16 v11, v11, v13\n"
    "v_pk_ashrrev_i16 v8, 15, v8 op_sel_hi:[0,1]\n"
    "v_mfma_f32_32x32x16_bf16 v[48:63], v[172:175], v[28:31], v[48:63]\n"
    "s_barrier\n"
    "ds_read_b128 v[176:179], v7 offset:0\n"
    "ds_read_b128 v[28:31], v240 offset:26624\n"
    "v_pk_ashrrev_i16 v9, 15, v9 op_sel_hi:[0,1]\n"
    "v_pk_ashrrev_i16 v10, 15, v10 op_sel_hi:[0,1]\n"
    "v_pk_ashrrev_i16 v11, 15, v11 op_sel_hi:[0,1]\n"
    "v_bitop3_b32 v188, v188, v8, v188 bitop3:0x30\n"
    "v_bitop3_b32 v189, v189, v9, v189 bitop3:0x30\n"
    "v_bitop3_b32 v190, v190, v10, v190 bitop3:0x30\n"
    "v_bitop3_b32 v191, v191, v11, v191 bitop3:0x30\n"
    "ds_read_b64_tr_b16 v[196:197], v6 offset:10240\n"
    "ds_read_b64_tr_b16 v[198:199], v6 offset:10752\n"
    "s_waitcnt lgkmcnt(5)\n"
    "v_mfma_f32_32x32x16_bf16 v[64:79], v[172:175], v[16:19], v[64:79]\n"
    "ds_read_b128 v[16:19], v245 offset:0\n"
    "v_mfma_f32_32x32x16_bf16 v[32:47], v[160:163], v[20:23], v[32:47]\n"
    "ds_read_b128 v[20:23], v245 offset:1024\n"
    "s_waitcnt lgkmcnt(4)\n"
    "v_mfma_f32_32x32x16_bf16 v[48:63], v[160:163], v[24:27], v[48:63]\n"
    "ds_read_b128 v[24:27], v245 offset:2048\n"
    "s_waitcnt lgkmcnt(3)\n"
    "v_and_b32 v8, s39, v196\n"
    "v_and_b32 v9, s39, v197\n"
    "v_and_b32 v10, s39, v198\n"
    "v_and_b32 v11, s39, v199\n"
    "v_pk_max_u16 v12, v8, v9\n"
    "v_pk_max_u16 v13, v10, v11\n"
    "v_pk_min_u16 v14, v8, v9\n"
    "v_mfma_f32_32x32x16_bf16 v[64:79], v[160:163], v[28:31], v[64:79]\n"
    "ds_read_b128 v[28:31], v246 offset:0\n"
    "v_pk_min_u16 v15, v10, v11\n"
    "v_pk_max_u16 v12, v12, v14 op_sel:[0,1] op_sel_hi:[1,0]\n"
    "v_pk_max_u16 v13, v13, v15 op_sel:[0,1] op_sel_hi:[1,0]\n"
    "v_pk_min_u16 v12, v12, v12 op_sel:[0,1] op_sel_hi:[1,0]\n"
    "v_pk_min_u16 v13, v13, v13 op_sel:[0,1] op_sel_hi:[1,0]\n"
    "v_pk_sub_i16 v8, v8, v12\n"
    "v_pk_sub_i16 v9, v9, v12\n"
    "v_pk_sub_i16 v10, v10, v13\n"
    "s_waitcnt lgkmcnt(2)\n"
    "v_mfma_f32_32x32x16_bf16 v[32:47], v[180:183], v[16:19], v[32:47]\n"
    "ds_read_b128 v[16:19], v246 offset:1024\n"
    "v_pk_sub_i16 v11, v11, v13\n"
    "ds_read_b64_tr_b16 v[200:201], v6 offset:12288\n"
    "ds_read_b64_tr_b16 v[202:203], v6 offset:12800\n"
    "v_pk_ashrrev_i16 v8, 15, v8 op_sel_hi:[0,1]\n"
    "v_pk_ashrrev_i16 v9, 15, v9 op_sel_hi:[0,1]\n"
    "v_pk_ashrrev_i16 v10, 15, v10 op_sel_hi:[0,1]\n"
    "v_pk_ashrrev_i16 v11, 15, v11 op_sel_hi:[0,1]\n"
    "v_bitop3_b32 v196, v196, v8, v196 bitop3:0x30\n"
    "v_bitop3_b32 v197, v197, v9, v197 bitop3:0x30\n"
    "v_mfma_f32_32x32x16_bf16 v[48:63], v[180:183], v[20:23], v[48:63]\n"
    "ds_read_b128 v[20:23], v246 offset:2048\n"
    "v_bitop3_b32 v198, v198, v10, v198 bitop3:0x30\n"
    "v_bitop3_b32 v199, v199, v11, v199 bitop3:0x30\n"
    "s_waitcnt lgkmcnt(1)\n"
    "v_and_b32 v8, s39, v200\n"
    "v_and_b32 v9, s39, v201\n"
    "v_and_b32 v10, s39, v202\n"
    "v_and_b32 v11, s39, v203\n"
    "v_mfma_f32_32x32x16_bf16 v[64:79], v[180:183], v[24:27], v[64:79]\n"
    "ds_read_b128 v[24:27], v247 offset:0\n"
    "v_pk_max_u16 v12, v8, v9\n"
    "v_pk_max_u16 v13, v10, v11\n"
    "v_pk_min_u16 v14, v8, v9\n"
    "v_pk_min_u16 v15, v10, v11\n"
    "v_pk_max_u16 v12, v12, v14 op_sel:[0,1] op_sel_hi:[1,0]\n"
    "v_pk_max_u16 v13, v13, v15 op_sel:[0,1] op_sel_hi:[1,0]\n"
    "v_pk_min_u16 v12, v12, v12 op_sel:[0,1] op_sel_hi:[1,0]\n"
    "v_pk_min_u16 v13, v13, v13 op_sel:[0,1] op_sel_hi:[1,0]\n"
    "v_mfma_f32_32x32x16_bf16 v[32:47], v[184:187], v[28:31], v[32:47]\n"
    "ds_read_b128 v[28:31], v247 offset:1024\n"
    "v_pk_sub_i16 v8, v8, v12\n"
    "v_pk_sub_i16 v9, v9, v12\n"
    "v_pk_sub_i16 v10, v10, v13\n"
    "v_pk_sub_i16 v11, v11, v13\n"
    "ds_read_b64_tr_b16 v[204:205], v6 offset:14336\n"
    "ds_read_b64_tr_b16 v[206:207], v6 offset:14848\n"
    "v_pk_ashrrev_i16 v8, 15, v8 op_sel_hi:[0,1]\n"
    "v_pk_ashrrev_i16 v9, 15, v9 op_sel_hi:[0,1]\n"
    "v_pk_ashrrev_i16 v10, 15, v10 op_sel_hi:[0,1]\n"
    "v_mfma_f32_32x32x16_bf16 v[48:63], v[184:187], v[16:19], v[48:63]\n"
    "ds_read_b128 v[16:19], v247 offset:2048\n"
    "v_pk_ashrrev_i16 v11, 15, v11 op_sel_hi:[0,1]\n"
    "v_bitop3_b32 v200, v200, v8, v200 bitop3:0x30\n"
    "v_bitop3_b32 v201, v201, v9, v201 bitop3:0x30\n"
    "v_bitop3_b32 v202, v202, v10, v202 bitop3:0x30\n"
    "v_bitop3_b32 v203, v203, v11, v203 bitop3:0x30\n"
    "s_waitcnt lgkmcnt(1)\n"
    "v_and_b32 v8, s39, v204\n"
    "v_mfma_f32_32x32x16_bf16 v[64:79], v[184:187], v[20:23], v[64:79]\n"
    "ds_read_b128 v[20:23], v244 offset:0\n"
    "v_and_b32 v9, s39, v205\n"
    "v_and_b32 v10, s39, v206\n"
    "v_and_b32_e64 v11, s39, v207\n"
    "v_pk_max_u16 v12, v8, v9\n"
    "v_pk_max_u16 v13, v10, v11\n"
    "v_pk_min_u16 v14, v8, v9\n"
    "v_pk_min_u16 v15, v10, v11\n"
    "v_pk_max_u16 v12, v12, v14 op_sel:[0,1] op_sel_hi:[1,0]\n"
    "v_mfma_f32_32x32x16_bf16 v[32:47], v[188:191], v[24:27], v[32:47]\n"
    "ds_read_b128 v[24:27], v244 offset:1024\n"
    "v_pk_max_u16 v13, v13, v15 op_sel:[0,1] op_sel_hi:[1,0]\n"
    "v_pk_min_u16 v12, v12, v12 op_sel:[0,1] op_sel_hi:[1,0]\n"
    "v_pk_min_u16 v13, v13, v13 op_sel:[0,1] op_sel_hi:[1,0]\n"
    "v_pk_sub_i16 v8, v8, v12\n"
    "v_pk_sub_i16 v9, v9, v12\n"
    "v_pk_sub_i16 v10, v10, v13\n"
    "v_pk_sub_i16 v11, v11, v13\n"
    "v_pk_ashrrev_i16 v8, 15, v8 op_sel_hi:[0,1]\n"
    "v_mfma_f32_32x32x16_bf16 v[48:63], v[188:191], v[28:31], v[48:63]\n"
    "s_waitcnt vmcnt(0)\n"
    "s_barrier\n"
    "ds_read_b128 v[192:195], v7 offset:4096\n"
    "ds_read_b128 v[28:31], v244 offset:2048\n"
    "v_pk_ashrrev_i16 v9, 15, v9 op_sel_hi:[0,1]\n"
    "v_pk_ashrrev_i16 v10, 15, v10 op_sel_hi:[0,1]\n"
    "v_pk_ashrrev_i16 v11, 15, v11 op_sel_hi:[0,1]\n"
    "v_bitop3_b32 v204, v204, v8, v204 bitop3:0x30\n"
    "v_bitop3_b32 v205, v205, v9, v205 bitop3:0x30\n"
    "v_bitop3_b32 v206, v206, v10, v206 bitop3:0x30\n"
    "v_bitop3_b32 v207, v207, v11, v207 bitop3:0x30\n"
    "ds_read_b64_tr_b16 v[116:117], v6 offset:18432\n"
    "ds_read_b64_tr_b16 v[118:119], v6 offset:18944\n"
    "s_waitcnt lgkmcnt(5)\n"
    "v_mfma_f32_32x32x16_bf16 v[64:79], v[188:191], v[16:19], v[64:79]\n"
    "ds_read_b128 v[16:19], v3 offset:0\n"
    "v_mfma_f32_32x32x16_bf16 v[32:47], v[176:179], v[20:23], v[32:47]\n"
    "ds_read_b128 v[20:23], v3 offset:1024\n"
    "s_waitcnt lgkmcnt(4)\n"
    "v_mfma_f32_32x32x16_bf16 v[48:63], v[176:179], v[24:27], v[48:63]\n"
    "ds_read_b128 v[24:27], v3 offset:2048\n"
    "s_waitcnt lgkmcnt(3)\n"
    "v_and_b32 v8, s39, v116\n"
    "v_and_b32 v9, s39, v117\n"
    "v_and_b32 v10, s39, v118\n"
    "v_and_b32_e64 v11, s39, v119\n"
    "v_pk_max_u16 v12, v8, v9\n"
    "v_pk_max_u16 v13, v10, v11\n"
    "v_pk_min_u16 v14, v8, v9\n"
    "v_mfma_f32_32x32x16_bf16 v[64:79], v[176:179], v[28:31], v[64:79]\n"
    "ds_read_b128 v[28:31], v4 offset:0\n"
    "v_pk_min_u16 v15, v10, v11\n"
    "v_pk_max_u16 v12, v12, v14 op_sel:[0,1] op_sel_hi:[1,0]\n"
    "v_pk_max_u16 v13, v13, v15 op_sel:[0,1] op_sel_hi:[1,0]\n"
    "v_pk_min_u16 v12, v12, v12 op_sel:[0,1] op_sel_hi:[1,0]\n"
    "v_pk_min_u16 v13, v13, v13 op_sel:[0,1] op_sel_hi:[1,0]\n"
    "v_pk_sub_i16 v8, v8, v12\n"
    "v_pk_sub_i16 v9, v9, v12\n"
    "v_pk_sub_i16 v10, v10, v13\n"
    "s_waitcnt lgkmcnt(2)\n"
    "v_mfma_f32_32x32x16_bf16 v[32:47], v[196:199], v[16:19], v[32:47]\n"
    "ds_read_b128 v[16:19], v4 offset:1024\n"
    "v_pk_sub_i16 v11, v11, v13\n"
    "ds_read_b64_tr_b16 v[120:121], v6 offset:20480\n"
    "ds_read_b64_tr_b16 v[122:123], v6 offset:20992\n"
    "v_pk_ashrrev_i16 v8, 15, v8 op_sel_hi:[0,1]\n"
    "v_pk_ashrrev_i16 v9, 15, v9 op_sel_hi:[0,1]\n"
    "v_pk_ashrrev_i16 v10, 15, v10 op_sel_hi:[0,1]\n"
    "v_pk_ashrrev_i16 v11, 15, v11 op_sel_hi:[0,1]\n"
    "v_bitop3_b32 v116, v116, v8, v116 bitop3:0x30\n"
    "v_bitop3_b32 v117, v117, v9, v117 bitop3:0x30\n"
    "v_mfma_f32_32x32x16_bf16 v[48:63], v[196:199], v[20:23], v[48:63]\n"
    "ds_read_b128 v[20:23], v4 offset:2048\n"
    "v_bitop3_b32 v118, v118, v10, v118 bitop3:0x30\n"
    "v_bitop3_b32 v119, v119, v11, v119 bitop3:0x30\n"
    "s_waitcnt lgkmcnt(1)\n"
    "v_and_b32 v8, s39, v120\n"
    "v_and_b32 v9, s39, v121\n"
    "v_and_b32 v10, s39, v122\n"
    "v_and_b32 v11, s39, v123\n"
    "v_mfma_f32_32x32x16_bf16 v[64:79], v[196:199], v[24:27], v[64:79]\n"
    "ds_read_b128 v[24:27], v5 offset:0\n"
    "v_pk_max_u16 v12, v8, v9\n"
    "v_pk_max_u16 v13, v10, v11\n"
    "v_pk_min_u16 v14, v8, v9\n"
    "v_pk_min_u16 v15, v10, v11\n"
    "v_pk_max_u16 v12, v12, v14 op_sel:[0,1] op_sel_hi:[1,0]\n"
    "v_pk_max_u16 v13, v13, v15 op_sel:[0,1] op_sel_hi:[1,0]\n"
    "v_pk_min_u16 v12, v12, v12 op_sel:[0,1] op_sel_hi:[1,0]\n"
    "v_pk_min_u16 v13, v13, v13 op_sel:[0,1] op_sel_hi:[1,0]\n"
    "v_mfma_f32_32x32x16_bf16 v[32:47], v[200:203], v[28:31], v[32:47]\n"
    "ds_read_b128 v[28:31], v5 offset:1024\n"
    "v_pk_sub_i16 v8, v8, v12\n"
    "v_pk_sub_i16 v9, v9, v12\n"
    "v_pk_sub_i16 v10, v10, v13\n"
    "v_pk_sub_i16 v11, v11, v13\n"
    "ds_read_b64_tr_b16 v[124:125], v6 offset:22528\n"
    "ds_read_b64_tr_b16 v[126:127], v6 offset:23040\n"
    "v_pk_ashrrev_i16 v8, 15, v8 op_sel_hi:[0,1]\n"
    "v_pk_ashrrev_i16 v9, 15, v9 op_sel_hi:[0,1]\n"
    "v_pk_ashrrev_i16 v10, 15, v10 op_sel_hi:[0,1]\n"
    "v_mfma_f32_32x32x16_bf16 v[48:63], v[200:203], v[16:19], v[48:63]\n"
    "ds_read_b128 v[16:19], v5 offset:2048\n"
    "v_pk_ashrrev_i16 v11, 15, v11 op_sel_hi:[0,1]\n"
    "v_bitop3_b32 v120, v120, v8, v120 bitop3:0x30\n"
    "v_bitop3_b32 v121, v121, v9, v121 bitop3:0x30\n"
    "v_bitop3_b32 v122, v122, v10, v122 bitop3:0x30\n"
    "v_bitop3_b32 v123, v123, v11, v123 bitop3:0x30\n"
    "s_waitcnt lgkmcnt(1)\n"
    "v_and_b32 v8, s39, v124\n"
    "v_mfma_f32_32x32x16_bf16 v[64:79], v[200:203], v[20:23], v[64:79]\n"
    "ds_read_b128 v[20:23], v2 offset:0\n"
    "v_and_b32 v9, s39, v125\n"
    "v_and_b32 v10, s39, v126\n"
    "v_and_b32_e64 v11, s39, v127\n"
    "v_pk_max_u16 v12, v8, v9\n"
    "v_pk_max_u16 v13, v10, v11\n"
    "v_pk_min_u16 v14, v8, v9\n"
    "v_pk_min_u16 v15, v10, v11\n"
    "v_pk_max_u16 v12, v12, v14 op_sel:[0,1] op_sel_hi:[1,0]\n"
    "v_mfma_f32_32x32x16_bf16 v[32:47], v[204:207], v[24:27], v[32:47]\n"
    "ds_read_b128 v[24:27], v2 offset:1024\n"
    "v_pk_max_u16 v13, v13, v15 op_sel:[0,1] op_sel_hi:[1,0]\n"
    "v_pk_min_u16 v12, v12, v12 op_sel:[0,1] op_sel_hi:[1,0]\n"
    "v_pk_min_u16 v13, v13, v13 op_sel:[0,1] op_sel_hi:[1,0]\n"
    "v_pk_sub_i16 v8, v8, v12\n"
    "v_pk_sub_i16 v9, v9, v12\n"
    "v_pk_sub_i16 v10, v10, v13\n"
    "v_pk_sub_i16 v11, v11, v13\n"
    "v_pk_ashrrev_i16 v8, 15, v8 op_sel_hi:[0,1]\n"
    "v_mfma_f32_32x32x16_bf16 v[48:63], v[204:207], v[28:31], v[48:63]\n"
    "s_barrier\n"
    "ds_read_b128 v[112:115], v7 offset:0\n"
    "ds_read_b128 v[28:31], v2 offset:2048\n"
    "v_pk_ashrrev_i16 v9, 15, v9 op_sel_hi:[0,1]\n"
    "v_pk_ashrrev_i16 v10, 15, v10 op_sel_hi:[0,1]\n"
    "v_pk_ashrrev_i16 v11, 15, v11 op_sel_hi:[0,1]\n"
    "v_bitop3_b32 v124, v124, v8, v124 bitop3:0x30\n"
    "v_bitop3_b32 v125, v125, v9, v125 bitop3:0x30\n"
    "v_bitop3_b32 v126, v126, v10, v126 bitop3:0x30\n"
    "v_bitop3_b32 v127, v127, v11, v127 bitop3:0x30\n"
    "ds_read_b64_tr_b16 v[132:133], v6 offset:26624\n"
    "ds_read_b64_tr_b16 v[134:135], v6 offset:27136\n"
    "s_waitcnt lgkmcnt(5)\n"
    "v_mfma_f32_32x32x16_bf16 v[64:79], v[204:207], v[16:19], v[64:79]\n"
    "ds_read_b128 v[16:19], v3 offset:24576\n"
    "v_mfma_f32_32x32x16_bf16 v[32:47], v[192:195], v[20:23], v[32:47]\n"
    "ds_read_b128 v[20:23], v3 offset:25600\n"
    "s_waitcnt lgkmcnt(4)\n"
    "v_mfma_f32_32x32x16_bf16 v[48:63], v[192:195], v[24:27], v[48:63]\n"
    "ds_read_b128 v[24:27], v3 offset:26624\n"
    "s_waitcnt lgkmcnt(3)\n"
    "v_and_b32 v8, s39, v132\n"
    "v_and_b32 v9, s39, v133\n"
    "v_and_b32 v10, s39, v134\n"
    "v_and_b32 v11, s39, v135\n"
    "v_pk_max_u16 v12, v8, v9\n"
    "v_pk_max_u16 v13, v10, v11\n"
    "v_pk_min_u16 v14, v8, v9\n"
    "v_mfma_f32_32x32x16_bf16 v[64:79], v[192:195], v[28:31], v[64:79]\n"
    "ds_read_b128 v[28:31], v4 offset:24576\n"
    "v_pk_min_u16 v15, v10, v11\n"
    "v_pk_max_u16 v12, v12, v14 op_sel:[0,1] op_sel_hi:[1,0]\n"
    "v_pk_max_u16 v13, v13, v15 op_sel:[0,1] op_sel_hi:[1,0]\n"
    "v_pk_min_u16 v12, v12, v12 op_sel:[0,1] op_sel_hi:[1,0]\n"
    "v_pk_min_u16 v13, v13, v13 op_sel:[0,1] op_sel_hi:[1,0]\n"
    "v_pk_sub_i16 v8, v8, v12\n"
    "v_pk_sub_i16 v9, v9, v12\n"
    "v_pk_sub_i16 v10, v10, v13\n"
    "s_waitcnt lgkmcnt(2)\n"
    "v_mfma_f32_32x32x16_bf16 v[32:47], v[116:119], v[16:19], v[32:47]\n"
    "ds_read_b128 v[16:19], v4 offset:25600\n"
    "v_pk_sub_i16 v11, v11, v13\n"
    "ds_read_b64_tr_b16 v[136:137], v6 offset:28672\n"
    "ds_read_b64_tr_b16 v[138:139], v6 offset:29184\n"
    "v_pk_ashrrev_i16 v8, 15, v8 op_sel_hi:[0,1]\n"
    "v_pk_ashrrev_i16 v9, 15, v9 op_sel_hi:[0,1]\n"
    "v_pk_ashrrev_i16 v10, 15, v10 op_sel_hi:[0,1]\n"
    "v_pk_ashrrev_i16 v11, 15, v11 op_sel_hi:[0,1]\n"
    "v_bitop3_b32 v132, v132, v8, v132 bitop3:0x30\n"
    "v_bitop3_b32 v133, v133, v9, v133 bitop3:0x30\n"
    "v_mfma_f32_32x32x16_bf16 v[48:63], v[116:119], v[20:23], v[48:63]\n"
    "ds_read_b128 v[20:23], v4 offset:26624\n"
    "v_bitop3_b32 v134, v134, v10, v134 bitop3:0x30\n"
    "v_bitop3_b32 v135, v135, v11, v135 bitop3:0x30\n"
    "s_waitcnt lgkmcnt(1)\n"
    "v_and_b32 v8, s39, v136\n"
    "v_and_b32 v9, s39, v137\n"
    "v_and_b32 v10, s39, v138\n"
    "v_and_b32 v11, s39, v139\n"
    "v_mfma_f32_32x32x16_bf16 v[64:79], v[116:119], v[24:27], v[64:79]\n"
    "ds_read_b128 v[24:27], v5 offset:24576\n"
    "v_pk_max_u16 v12, v8, v9\n"
    "v_pk_max_u16 v13, v10, v11\n"
    "v_pk_min_u16 v14, v8, v9\n"
    "v_pk_min_u16 v15, v10, v11\n"
    "v_pk_max_u16 v12, v12, v14 op_sel:[0,1] op_sel_hi:[1,0]\n"
    "v_pk_max_u16 v13, v13, v15 op_sel:[0,1] op_sel_hi:[1,0]\n"
    "v_pk_min_u16 v12, v12, v12 op_sel:[0,1] op_sel_hi:[1,0]\n"
    "v_pk_min_u16 v13, v13, v13 op_sel:[0,1] op_sel_hi:[1,0]\n"
    "v_mfma_f32_32x32x16_bf16 v[32:47], v[120:123], v[28:31], v[32:47]\n"
    "ds_read_b128 v[28:31], v5 offset:25600\n"
    "v_pk_sub_i16 v8, v8, v12\n"
    "v_pk_sub_i16 v9, v9, v12\n"
    "v_pk_sub_i16 v10, v10, v13\n"
    "v_pk_sub_i16 v11, v11, v13\n"
    "ds_read_b64_tr_b16 v[140:141], v6 offset:30720\n"
    "ds_read_b64_tr_b16 v[142:143], v6 offset:31232\n"
    "v_pk_ashrrev_i16 v8, 15, v8 op_sel_hi:[0,1]\n"
    "v_pk_ashrrev_i16 v9, 15, v9 op_sel_hi:[0,1]\n"
    "v_pk_ashrrev_i16 v10, 15, v10 op_sel_hi:[0,1]\n"
    "v_mfma_f32_32x32x16_bf16 v[48:63], v[120:123], v[16:19], v[48:63]\n"
    "ds_read_b128 v[16:19], v5 offset:26624\n"
    "v_pk_ashrrev_i16 v11, 15, v11 op_sel_hi:[0,1]\n"
    "v_bitop3_b32 v136, v136, v8, v136 bitop3:0x30\n"
    "v_bitop3_b32 v137, v137, v9, v137 bitop3:0x30\n"
    "v_bitop3_b32 v138, v138, v10, v138 bitop3:0x30\n"
    "v_bitop3_b32 v139, v139, v11, v139 bitop3:0x30\n"
    "s_waitcnt lgkmcnt(1)\n"
    "v_and_b32 v8, s39, v140\n"
    "v_mfma_f32_32x32x16_bf16 v[64:79], v[120:123], v[20:23], v[64:79]\n"
    "ds_read_b128 v[20:23], v2 offset:24576\n"
    "v_and_b32 v9, s39, v141\n"
    "v_and_b32 v10, s39, v142\n"
    "v_and_b32_e64 v11, s39, v143\n"
    "v_pk_max_u16 v12, v8, v9\n"
    "v_pk_max_u16 v13, v10, v11\n"
    "v_pk_min_u16 v14, v8, v9\n"
    "v_pk_min_u16 v15, v10, v11\n"
    "v_pk_max_u16 v12, v12, v14 op_sel:[0,1] op_sel_hi:[1,0]\n"
    "v_mfma_f32_32x32x16_bf16 v[32:47], v[124:127], v[24:27], v[32:47]\n"
    "ds_read_b128 v[24:27], v2 offset:25600\n"
    "v_pk_max_u16 v13, v13, v15 op_sel:[0,1] op_sel_hi:[1,0]\n"
    "v_pk_min_u16 v12, v12, v12 op_sel:[0,1] op_sel_hi:[1,0]\n"
    "v_pk_min_u16 v13, v13, v13 op_sel:[0,1] op_sel_hi:[1,0]\n"
    "v_pk_sub_i16 v8, v8, v12\n"
    "v_pk_sub_i16 v9, v9, v12\n"
    "v_pk_sub_i16 v10, v10, v13\n"
    "v_pk_sub_i16 v11, v11, v13\n"
    "v_pk_ashrrev_i16 v8, 15, v8 op_sel_hi:[0,1]\n"
    "v_mfma_f32_32x32x16_bf16 v[48:63], v[124:127], v[28:31], v[48:63]\n"
    "s_barrier\n"
    "ds_read_b128 v[128:131], v7 offset:4096\n"
    "ds_read_b128 v[28:31], v2 offset:26624\n"
    "v_pk_ashrrev_i16 v9, 15, v9 op_sel_hi:[0,1]\n"
    "v_pk_ashrrev_i16 v10, 15, v10 op_sel_hi:[0,1]\n"
    "v_pk_ashrrev_i16 v11, 15, v11 op_sel_hi:[0,1]\n"
    "v_bitop3_b32 v140, v140, v8, v140 bitop3:0x30\n"
    "v_bitop3_b32 v141, v141, v9, v141 bitop3:0x30\n"
    "v_bitop3_b32 v142, v142, v10, v142 bitop3:0x30\n"
    "v_bitop3_b32 v143, v143, v11, v143 bitop3:0x30\n"
    "ds_read_b64_tr_b16 v[148:149], v6 offset:2048\n"
    "ds_read_b64_tr_b16 v[150:151], v6 offset:2560\n"
    "s_waitcnt lgkmcnt(5)\n"
    "v_mfma_f32_32x32x16_bf16 v[64:79], v[124:127], v[16:19], v[64:79]\n"
    "ds_read_b128 v[16:19], v241 offset:0\n"
    "v_mfma_f32_32x32x16_bf16 v[32:47], v[112:115], v[20:23], v[32:47]\n"
    "ds_read_b128 v[20:23], v241 offset:1024\n"
    "s_waitcnt lgkmcnt(4)\n"
    "v_mfma_f32_32x32x16_bf16 v[48:63], v[112:115], v[24:27], v[48:63]\n"
    "ds_read_b128 v[24:27], v241 offset:2048\n"
    "s_waitcnt lgkmcnt(3)\n"
    "v_and_b32 v8, s39, v148\n"
    "v_and_b32 v9, s39, v149\n"
    "v_and_b32 v10, s39, v150\n"
    "v_and_b32 v11, s39, v151\n"
    "v_pk_max_u16 v12, v8, v9\n"
    "v_pk_max_u16 v13, v10, v11\n"
    "v_pk_min_u16 v14, v8, v9\n"
    "v_mfma_f32_32x32x16_bf16 v[64:79], v[112:115], v[28:31], v[64:79]\n"
    "ds_read_b128 v[28:31], v242 offset:0\n"
    "v_pk_min_u16 v15, v10, v11\n"
    "v_pk_max_u16 v12, v12, v14 op_sel:[0,1] op_sel_hi:[1,0]\n"
    "v_pk_max_u16 v13, v13, v15 op_sel:[0,1] op_sel_hi:[1,0]\n"
    "v_pk_min_u16 v12, v12, v12 op_sel:[0,1] op_sel_hi:[1,0]\n"
    "v_pk_min_u16 v13, v13, v13 op_sel:[0,1] op_sel_hi:[1,0]\n"
    "v_pk_sub_i16 v8, v8, v12\n"
    "v_pk_sub_i16 v9, v9, v12\n"
    "v_pk_sub_i16 v10, v10, v13\n"
    "s_waitcnt lgkmcnt(2)\n"
    "v_mfma_f32_32x32x16_bf16 v[32:47], v[132:135], v[16:19], v[32:47]\n"
    "ds_read_b128 v[16:19], v242 offset:1024\n"
    "v_pk_sub_i16 v11, v11, v13\n"
    "ds_read_b64_tr_b16 v[152:153], v6 offset:4096\n"
    "ds_read_b64_tr_b16 v[154:155], v6 offset:4608\n"
    "v_pk_ashrrev_i16 v8, 15, v8 op_sel_hi:[0,1]\n"
    "v_pk_ashrrev_i16 v9, 15, v9 op_sel_hi:[0,1]\n"
    "v_pk_ashrrev_i16 v10, 15, v10 op_sel_hi:[0,1]\n"
    "v_pk_ashrrev_i16 v11, 15, v11 op_sel_hi:[0,1]\n"
    "v_bitop3_b32 v148, v148, v8, v148 bitop3:0x30\n"
    "v_bitop3_b32 v149, v149, v9, v149 bitop3:0x30\n"
    "v_mfma_f32_32x32x16_bf16 v[48:63], v[132:135], v[20:23], v[48:63]\n"
    "ds_read_b128 v[20:23], v242 offset:2048\n"
    "v_bitop3_b32 v150, v150, v10, v150 bitop3:0x30\n"
    "v_bitop3_b32 v151, v151, v11, v151 bitop3:0x30\n"
    "s_waitcnt lgkmcnt(1)\n"
    "v_and_b32 v8, s39, v152\n"
    "v_and_b32 v9, s39, v153\n"
    "v_and_b32 v10, s39, v154\n"
    "v_and_b32 v11, s39, v155\n"
    "v_mfma_f32_32x32x16_bf16 v[64:79], v[132:135], v[24:27], v[64:79]\n"
    "ds_read_b128 v[24:27], v243 offset:0\n"
    "v_pk_max_u16 v12, v8, v9\n"
    "v_pk_max_u16 v13, v10, v11\n"
    "v_pk_min_u16 v14, v8, v9\n"
    "v_pk_min_u16 v15, v10, v11\n"
    "v_pk_max_u16 v12, v12, v14 op_sel:[0,1] op_sel_hi:[1,0]\n"
    "v_pk_max_u16 v13, v13, v15 op_sel:[0,1] op_sel_hi:[1,0]\n"
    "v_pk_min_u16 v12, v12, v12 op_sel:[0,1] op_sel_hi:[1,0]\n"
    "v_pk_min_u16 v13, v13, v13 op_sel:[0,1] op_sel_hi:[1,0]\n"
    "v_mfma_f32_32x32x16_bf16 v[32:47], v[136:139], v[28:31], v[32:47]\n"
    "ds_read_b128 v[28:31], v243 offset:1024\n"
    "v_pk_sub_i16 v8, v8, v12\n"
    "v_pk_sub_i16 v9, v9, v12\n"
    "v_pk_sub_i16 v10, v10, v13\n"
    "v_pk_sub_i16 v11, v11, v13\n"
    "ds_read_b64_tr_b16 v[156:157], v6 offset:6144\n"
    "ds_read_b64_tr_b16 v[158:159], v6 offset:6656\n"
    "v_pk_ashrrev_i16 v8, 15, v8 op_sel_hi:[0,1]\n"
    "v_pk_ashrrev_i16 v9, 15, v9 op_sel_hi:[0,1]\n"
    "v_pk_ashrrev_i16 v10, 15, v10 op_sel_hi:[0,1]\n"
    "v_mfma_f32_32x32x16_bf16 v[48:63], v[136:139], v[16:19], v[48:63]\n"
    "ds_read_b128 v[16:19], v243 offset:2048\n"
    "v_pk_ashrrev_i16 v11, 15, v11 op_sel_hi:[0,1]\n"
    "v_bitop3_b32 v152, v152, v8, v152 bitop3:0x30\n"
    "v_bitop3_b32 v153, v153, v9, v153 bitop3:0x30\n"
    "v_bitop3_b32 v154, v154, v10, v154 bitop3:0x30\n"
    "v_bitop3_b32 v155, v155, v11, v155 bitop3:0x30\n"
    "s_waitcnt lgkmcnt(1)\n"
    "v_and_b32 v8, s39, v156\n"
    "v_mfma_f32_32x32x16_bf16 v[64:79], v[136:139], v[20:23], v[64:79]\n"
    "ds_read_b128 v[20:23], v240 offset:0\n"
    "v_and_b32 v9, s39, v157\n"
    "v_and_b32 v10, s39, v158\n"
    "v_and_b32_e64 v11, s39, v159\n"
    "v_pk_max_u16 v12, v8, v9\n"
    "v_pk_max_u16 v13, v10, v11\n"
    "v_pk_min_u16 v14, v8, v9\n"
    "v_pk_min_u16 v15, v10, v11\n"
    "v_pk_max_u16 v12, v12, v14 op_sel:[0,1] op_sel_hi:[1,0]\n"
    "v_mfma_f32_32x32x16_bf16 v[32:47], v[140:143], v[24:27], v[32:47]\n"
    "ds_read_b128 v[24:27], v240 offset:1024\n"
    "v_pk_max_u16 v13, v13, v15 op_sel:[0,1] op_sel_hi:[1,0]\n"
    "v_pk_min_u16 v12, v12, v12 op_sel:[0,1] op_sel_hi:[1,0]\n"
    "v_pk_min_u16 v13, v13, v13 op_sel:[0,1] op_sel_hi:[1,0]\n"
    "v_pk_sub_i16 v8, v8, v12\n"
    "v_pk_sub_i16 v9, v9, v12\n"
    "v_pk_sub_i16 v10, v10, v13\n"
    "v_pk_sub_i16 v11, v11, v13\n"
    "v_pk_ashrrev_i16 v8, 15, v8 op_sel_hi:[0,1]\n"
    "v_mfma_f32_32x32x16_bf16 v[48:63], v[140:143], v[28:31], v[48:63]\n"
    "s_barrier\n"
    "ds_read_b128 v[144:147], v7 offset:0\n"
    "ds_read_b128 v[28:31], v240 offset:2048\n"
    "v_pk_ashrrev_i16 v9, 15, v9 op_sel_hi:[0,1]\n"
    "v_pk_ashrrev_i16 v10, 15, v10 op_sel_hi:[0,1]\n"
    "v_pk_ashrrev_i16 v11, 15, v11 op_sel_hi:[0,1]\n"
    "v_bitop3_b32 v156, v156, v8, v156 bitop3:0x30\n"
    "v_bitop3_b32 v157, v157, v9, v157 bitop3:0x30\n"
    "v_bitop3_b32 v158, v158, v10, v158 bitop3:0x30\n"
    "v_bitop3_b32 v159, v159, v11, v159 bitop3:0x30\n"
    "s_waitcnt lgkmcnt(3)\n"
    "v_mfma_f32_32x32x16_bf16 v[64:79], v[140:143], v[16:19], v[64:79]\n"
    "ds_read_b128 v[16:19], v241 offset:24576\n"
    "s_mov_b32 s28, s30\n"
    "v_lshrrev_b32 v10, 3, v1\n"
    "v_xor_b32 v10, v10, v1\n"
    "v_and_b32 v10, 3, v10\n"
    "v_mfma_f32_32x32x16_bf16 v[32:47], v[128:131], v[20:23], v[32:47]\n"
    "ds_read_b128 v[20:23], v241 offset:25600\n"
    "v_lshlrev_b32 v10, 4, v10\n"
    "s_waitcnt lgkmcnt(2)\n"
    "v_mfma_f32_32x32x16_bf16 v[48:63], v[128:131], v[24:27], v[48:63]\n"
    "ds_read_b128 v[24:27], v241 offset:26624\n"
    "v_lshrrev_b32_e64 v11, 2, v1\n"
    "v_mfma_f32_32x32x16_bf16 v[64:79], v[128:131], v[28:31], v[64:79]\n"
    "ds_read_b128 v[28:31], v242 offset:24576\n"
    "s_waitcnt lgkmcnt(2)\n"
    "v_mfma_f32_32x32x16_bf16 v[32:47], v[148:151], v[16:19], v[32:47]\n"
    "ds_read_b128 v[16:19], v242 offset:25600\n"
    "v_lshl_add_u32 v10, v11, 6, v10\n"
    "v_mfma_f32_32x32x16_bf16 v[48:63], v[148:151], v[20:23], v[48:63]\n"
    "ds_read_b128 v[20:23], v242 offset:26624\n"
    "s_waitcnt lgkmcnt(2)\n"
    "v_mfma_f32_32x32x16_bf16 v[64:79], v[148:151], v[24:27], v[64:79]\n"
    "ds_read_b128 v[24:27], v243 offset:24576\n"
    "v_add_u32_e64 v8, s28, v10\n"
    "v_mfma_f32_32x32x16_bf16 v[32:47], v[152:155], v[28:31], v[32:47]\n"
    "ds_read_b128 v[28:31], v243 offset:25600\n"
    "v_mul_u32_u24 v11, 0x480, v11\n"
    "s_waitcnt lgkmcnt(2)\n"
    "v_mfma_f32_32x32x16_bf16 v[48:63], v[152:155], v[16:19], v[48:63]\n"
    "ds_read_b128 v[16:19], v243 offset:26624\n"
    "v_mfma_f32_32x32x16_bf16 v[64:79], v[152:155], v[20:23], v[64:79]\n"
    "ds_read_b128 v[20:23], v240 offset:24576\n"
    "v_and_b32_e64 v10, 3, v1\n"
    "s_waitcnt lgkmcnt(2)\n"
    "v_mfma_f32_32x32x16_bf16 v[32:47], v[156:159], v[24:27], v[32:47]\n"
    "ds_read_b128 v[24:27], v240 offset:25600\n"
    "v_mfma_f32_32x32x16_bf16 v[48:63], v[156:159], v[28:31], v[48:63]\n"
    "ds_read_b128 v[28:31], v240 offset:26624\n"
    "v_lshl_add_u32 v11, v10, 4, v11\n"
    "s_waitcnt lgkmcnt(2)\n"
    "v_mfma_f32_32x32x16_bf16 v[64:79], v[156:159], v[16:19], v[64:79]\n"
    "v_add_u32 v9, s25, v11\n"
    "v_mfma_f32_32x32x16_bf16 v[32:47], v[144:147], v[20:23], v[32:47]\n"
    "s_waitcnt lgkmcnt(1)\n"
    "v_mfma_f32_32x32x16_bf16 v[48:63], v[144:147], v[24:27], v[48:63]\n"
    "v_xor_b32_e64 v1, 16, v0\n"
    "s_waitcnt lgkmcnt(0)\n"
    "v_mfma_f32_32x32x16_bf16 v[64:79], v[144:147], v[28:31], v[64:79]\n"
    "s_nop 6\n"
    "v_cvt_pk_bf16_f32 v32, v32, v33\n"
    "v_cvt_pk_bf16_f32 v33, v34, v35\n"
    "v_cvt_pk_bf16_f32 v34, v36, v37\n"
    "v_cvt_pk_bf16_f32 v35, v38, v39\n"
    "ds_write_b128 v0, v[32:35] offset:0\n"
    "v_cvt_pk_bf16_f32 v36, v40, v41\n"
    "v_cvt_pk_bf16_f32 v37, v42, v43\n"
    "v_cvt_pk_bf16_f32 v38, v44, v45\n"
    "v_cvt_pk_bf16_f32 v39, v46, v47\n"
    "ds_write_b128 v1, v[36:39] offset:0\n"
    "v_cvt_pk_bf16_f32 v48, v48, v49\n"
    "v_cvt_pk_bf16_f32 v49, v50, v51\n"
    "v_cvt_pk_bf16_f32 v50, v52, v53\n"
    "v_cvt_pk_bf16_f32 v51, v54, v55\n"
    "ds_write_b128 v0, v[48:51] offset:8192\n"
    "v_cvt_pk_bf16_f32 v52, v56, v57\n"
    "v_cvt_pk_bf16_f32 v53, v58, v59\n"
    "v_cvt_pk_bf16_f32 v54, v60, v61\n"
    "v_cvt_pk_bf16_f32 v55, v62, v63\n"
    "ds_write_b128 v1, v[52:55] offset:8192\n"
    "v_cvt_pk_bf16_f32 v64, v64, v65\n"
    "v_cvt_pk_bf16_f32 v65, v66, v67\n"
    "v_cvt_pk_bf16_f32 v66, v68, v69\n"
    "v_cvt_pk_bf16_f32 v67, v70, v71\n"
    "ds_write_b128 v0, v[64:67] offset:16384\n"
    "v_cvt_pk_bf16_f32 v68, v72, v73\n"
    "v_cvt_pk_bf16_f32 v69, v74, v75\n"
    "v_cvt_pk_bf16_f32 v70, v76, v77\n"
    "v_cvt_pk_bf16_f32 v71, v78, v79\n"
    "ds_write_b128 v1, v[68:71] offset:16384\n"
    "ds_read_b128 v[40:43], v8 offset:0\n"
    "ds_read_b128 v[44:47], v8 offset:1024\n"
    "s_add_u32 s28, s36, 0x0\n"
    "s_waitcnt lgkmcnt(1)\n"
    "buffer_store_dwordx4 v[40:43], v9, s[16:19], s28 offen nt\n"
    "s_add_u32 s28, s36, 0x4800\n"
    "s_waitcnt lgkmcnt(0)\n"
    "buffer_store_dwordx4 v[44:47], v9, s[16:19], s28 offen nt\n"
    "ds_read_b128 v[56:59], v8 offset:8192\n"
    "ds_read_b128 v[60:63], v8 offset:9216\n"
    "s_add_u32 s28, s36, 0x9000\n"
    "s_waitcnt lgkmcnt(1)\n"
    "buffer_store_dwordx4 v[56:59], v9, s[16:19], s28 offen nt\n"
    "s_add_u32 s28, s36, 0xd800\n"
    "s_waitcnt lgkmcnt(0)\n"
    "buffer_store_dwordx4 v[60:63], v9, s[16:19], s28 offen nt\n"
    "ds_read_b128 v[72:75], v8 offset:16384\n"
    "ds_read_b128 v[76:79], v8 offset:17408\n"
    "s_add_u32 s28, s36, 0x12000\n"
    "s_waitcnt lgkmcnt(1)\n"
    "buffer_store_dwordx4 v[72:75], v9, s[16:19], s28 offen nt\n"
    "s_add_u32 s28, s36, 0x16800\n"
    "s_waitcnt lgkmcnt(0)\n"
    "buffer_store_dwordx4 v[76:79], v9, s[16:19], s28 offen nt\n"
    "s_branch .Lend%=\n"
    ".Lend%=:\n"
    : "+v"(tid) : "s"(__builtin_amdgcn_kernarg_segment_ptr()), "s"(blockIdx.x), "s"((unsigned)(size_t)smem)
    : "memory", "vcc", "scc", "s4", "s5", "s6", "s7", "s8", "s9", "s10", "s11", "s12", "s13", "s14", "s15", "s16", "s17", "s18", "s19", "s20", "s21", "s22", "s23", "s24", "s25", "s26", "s27", "s28", "s29", "s30", "s31", "s33", "s34", "s35", "s36", "s37", "s38", "s39", "s40", "s41", "s42", "s43", "s44", "s45", "s46", "s47", "s48", "s49", "s50", "s51", "s52", "s53", "s54", "s55", "s56", "s57", "s58", "s59", "s60", "s61", "s62", "s63", "s64", "s65", "s66", "s67", "s68", "s69", "s70", "s71", "s72", "s73", "s74", "s75", "s76", "s77", "s78", "s79", "s80", "s81", "s82", "s83", "s84", "s85", "s86", "s87", "s88", "s89", "s90", "s91", "s92", "s93", "s94", "s95", "s96", "s97", "s98", "s99", "v1", "v2", "v3", "v4", "v5", "v6", "v7", "v8", "v9", "v10", "v11", "v12", "v13", "v14", "v15", "v16", "v17", "v18", "v19", "v20", "v21", "v22", "v23", "v24", "v25", "v26", "v27", "v28", "v29", "v30", "v31", "v32", "v33", "v34", "v35", "v36", "v37", "v38", "v39", "v40", "v41", "v42", "v43", "v44", "v45", "v46", "v47", "v48", "v49", "v50", "v51", "v52", "v53", "v54", "v55", "v56", "v57", "v58", "v59", "v60", "v61", "v62", "v63", "v64", "v65", "v66", "v67", "v68", "v69", "v70", "v71", "v72", "v73", "v74", "v75", "v76", "v77", "v78", "v79", "v80", "v81", "v82", "v83", "v84", "v85", "v86", "v87", "v88", "v89", "v90", "v91", "v92", "v93", "v94", "v95", "v96", "v97", "v98", "v99", "v100", "v101", "v102", "v103", "v104", "v105", "v106", "v107", "v108", "v109", "v110", "v111", "v112", "v113", "v114", "v115", "v116", "v117", "v118", "v119", "v120", "v121", "v122", "v123", "v124", "v125", "v126", "v127", "v128", "v129", "v130", "v131", "v132", "v133", "v134", "v135", "v136", "v137", "v138", "v139", "v140", "v141", "v142", "v143", "v144", "v145", "v146", "v147", "v148", "v149", "v150", "v151", "v152", "v153", "v154", "v155", "v156", "v157", "v158", "v159", "v160", "v161", "v162", "v163", "v164", "v165", "v166", "v167", "v168", "v169", "v170", "v171", "v172", "v173", "v174", "v175", "v176", "v177", "v178", "v179", "v180", "v181", "v182", "v183", "v184", "v185", "v186", "v187", "v188", "v189", "v190", "v191", "v192", "v193", "v194", "v195", "v196", "v197", "v198", "v199", "v200", "v201", "v202", "v203", "v204", "v205", "v206", "v207", "v208", "v209", "v210", "v211", "v212", "v213", "v214", "v215", "v216", "v217", "v218", "v219", "v220", "v221", "v222", "v223", "v224", "v225", "v226", "v227", "v228", "v229", "v230", "v231", "v232", "v233", "v234", "v235", "v236", "v237", "v238", "v239", "v240", "v241", "v242", "v243", "v244", "v245", "v246", "v247", "v248", "v249", "v250", "v251", "v252", "v253", "v254", "v255");
}

extern "C" void kernel_launch(void* const* d_in, const int* in_sizes, int n_in, void* d_out, int out_size, void* d_ws, size_t ws_size, hipStream_t stream) {
  spmm24_kernel<<<256, 512, 0, stream>>>((const uint16_t*)d_in[0], (const uint16_t*)d_in[1], (const uint16_t*)d_in[2], (uint16_t*)d_out);
}
```
